```python
import torch

def _softmax(x, axis=-1):
    return torch.softmax(x, dim=axis)
def _gelu(x):
    return torch.nn.functional.gelu(x, approximate="tanh")
def _silu(x):
    return torch.nn.functional.silu(x)
def _sigmoid(x):
    return torch.sigmoid(x)
def _layernorm(x, axis=-1, eps=1e-5):
    m = x.mean(dim=axis, keepdim=True)
    v = x.var(dim=axis, keepdim=True, unbiased=False)
    return (x - m) / torch.sqrt(v + eps)
def _scatter_add(tgt, idx, src, axis=0):
    return tgt.clone().index_add_(axis, idx, src)
def _segment_sum(src, seg, n):
    out = torch.zeros((n,) + src.shape[1:], dtype=src.dtype)
    return out.index_add_(0, seg, src)
def _pad(x, pw):
    flat = tuple(v for pair in reversed(pw) for v in pair)
    return torch.nn.functional.pad(x, flat)

kernel_name = 'standalone__self_sup_conv_stem_wavlm__speech'

def setup_inputs():

    _w = (lambda a: (lambda t: t.to(torch.bfloat16) if t.is_floating_point() else t)(torch.as_tensor(a)))
    B, T, D, K = 2, 16000, 256, 3
    scale = (K * D) ** -0.5
    return {
        "x": _w((lambda *s: torch.randn(*s, dtype=torch.bfloat16) * 0.05)(B, T, D)),
        "w": _w((lambda *s: torch.randn(*s, dtype=torch.bfloat16) * 0.05)(K, D, D) * scale),
        "gn_gamma": _w((lambda *s: torch.randn(*s, dtype=torch.bfloat16) * 0.05)(D) * 0.1 + 1.0),
        "gn_beta": _w((lambda *s: torch.randn(*s, dtype=torch.bfloat16) * 0.05)(D) * 0.1),
    }

def reference(x, w, gn_gamma, gn_beta):
    B, T, D = x.shape
    K = w.shape[0]
    s = 2
    L = (T - K) // s + 1
    h = torch.einsum('blc,cd->bld', x[:, 0:s * L:s, :], w[0])
    for i in range(1, K):
        h = h + torch.einsum('blc,cd->bld', x[:, i:i + s * L:s, :], w[i])
    mu = torch.einsum('bld->bd', h)[:, None, :] / L
    var = torch.einsum('bld->bd', (h - mu) ** 2)[:, None, :] / L
    h = (h - mu) / torch.sqrt(var + 1e-5)
    h = h * gn_gamma[None, None, :] + gn_beta[None, None, :]
    return _gelu(h)
```

```cpp
#include <hip/hip_runtime.h>
#include <hip/hip_bf16.h>
#include <utility>

typedef unsigned short u16;
typedef unsigned int u32;
typedef unsigned long long u64;
typedef __attribute__((ext_vector_type(4))) short s16x4;
typedef __attribute__((ext_vector_type(8))) __bf16 bf16x8;
typedef __attribute__((ext_vector_type(2))) __bf16 bf16x2;
typedef __attribute__((ext_vector_type(4))) float f32x4;
typedef __attribute__((ext_vector_type(2))) float f32x2;
typedef __attribute__((ext_vector_type(4))) u32 u32x4;
typedef __attribute__((ext_vector_type(2))) u32 u32x2;
#define LAS __attribute__((address_space(3)))
#define GAS __attribute__((address_space(1)))

constexpr int T_ = 16000, L_ = 7999;
constexpr int TR = 250;
constexpr int SLOT = 48 * 1024;
constexpr int RED_OFF = 3 * SLOT;
constexpr int AB_OFF = RED_OFF + 2048;
constexpr int FLG_OFF = AB_OFF + 512;
constexpr int PF_OFF = FLG_OFF + 256;
constexpr int LDS_BYTES = PF_OFF + 1280;
constexpr int WS_EPOCH = 0;
constexpr int WS_GRAN = 4096;
constexpr u32 OOB = 0x40000000u;
#define POLL_DELAY 16
#define OUT_AUX 16
#define POLL_DELAY0 20
#define EPRIO 0
#define G_ST 52
#define G_W 7
#define GPRIO 0
#define XAUX0 2
#define XAUX1 0
#define POLL_SLEEP 1
#define PREWRITE 0

#define STAMP(i) do {} while (0)
#define SB() __builtin_amdgcn_sched_barrier(0)
#define PIN(v) asm volatile("" : "+v"(v))
#define WAIT_LGKM(n) __builtin_amdgcn_s_waitcnt(0xC07F | ((n) << 8))
__device__ __forceinline__ void bar() { __builtin_amdgcn_s_barrier(); }
__device__ __forceinline__ float bf2f(u32 v) { return __uint_as_float(v << 16); }
__device__ __forceinline__ bf16x8 cat(s16x4 a, s16x4 b) { return __builtin_bit_cast(bf16x8, __builtin_shufflevector(a, b, 0, 1, 2, 3, 4, 5, 6, 7)); }
__device__ __forceinline__ void lds_wait_ge(int a, u32 v) {
#pragma unroll 1
  for (u32 sp = 0; sp < (1u << 22); ++sp) {
    u32 c; asm volatile("ds_read_b32 %0, %1\n\ts_waitcnt lgkmcnt(0)" : "=v"(c) : "v"(a) : "memory");
    if ((u32)__builtin_amdgcn_readfirstlane(c) >= v) break;
    __builtin_amdgcn_s_sleep(1);
  }
}

#define TJ(e) ((e) < 4 ? (e) : ((e) == 4 ? 0 : ((e) < 7 ? (e) - 1 : (e) - 6)))
#define TH(e) ((e) == 4 || (e) >= 7 ? 1 : 0)
template <int... I, class F> __device__ __forceinline__ void sfor_(std::integer_sequence<int, I...>, F&& f) { (f(std::integral_constant<int, I>{}), ...); }
template <int N, class F> __device__ __forceinline__ void sfor(F&& f) { sfor_(std::make_integer_sequence<int, N>{}, f); }
#define CI(x) std::integral_constant<int, (x)>{}
struct Frags { bf16x8 a[4]; s16x4 b[4][2]; };

__global__ __launch_bounds__(512, 2) void conv_stem_kernel(const u16* __restrict__ x, const u16* __restrict__ w,
                                                           const u16* __restrict__ gam, const u16* __restrict__ bet,
                                                           u16* __restrict__ out, char* __restrict__ ws
) {
  extern __shared__ __attribute__((aligned(16))) char smem_[];
  LAS char* smem = (LAS char*)smem_;
  asm volatile(".Lk_start:");
  const int bid = blockIdx.x;
  const int cg = (bid >> 3) & 3;
  const int rt = (bid & 7) * 8 + (bid >> 5);
  const int b = rt >> 5, t = rt & 31;
  const int l0 = t * TR;
  const int nvalid = (L_ - l0) < TR ? (L_ - l0) : TR;
  const int grp = b * 4 + cg;
  const int wid = __builtin_amdgcn_readfirstlane(threadIdx.x >> 6);
  const int lane = threadIdx.x & 63;
  const int wq = wid & 3;
  int h0, h1, abo, abf, ov;
  f32x4 hv[4][2];
  int xv = wq * 8192 + (lane >> 3) * 1024 + (((lane & 7) ^ (lane >> 4) ^ ((wq & 1) << 2)) << 4);
  int wv = wq * 4096 + (lane >> 3) * 512 + (((lane & 7) ^ (((lane >> 4) & 1) << 1) ^ ((wq & 1) << 2)) << 4);
  u32 xoff = (u32)(b * T_ + 2 * l0) * 512u, xlen = (u32)(2 * nvalid + 1) * 512u;
  int pfv = lane < 24 ? ((bid >> 5) * 96 + wq * 24 + lane) * 512 : (int)OOB;
  PIN(xv); PIN(wv); PIN(pfv);
  asm volatile("" : "+s"(xoff), "+s"(xlen));
  SB();
  asm volatile("" :: "s"(x), "s"(w), "s"(gam), "s"(bet), "s"(out), "s"(ws));
  u16* const outp = out + ((size_t)(b * L_ + l0) * 256 + cg * 64);
  auto epi_addr = [&]() {
    const int hf = wid < 4 ? 1 : 0;
    const int rr = lane >> 2, cc = lane & 3, sw = ((rr >> 1) & 1) | (((rr >> 2) & 1) << 3);
    const int hb = (wq >> 1) * 2 * SLOT + (wq & 1) * 16384 + rr * 256;
    h0 = hb + (((8 * hf + 2 * cc) ^ sw) << 4); h1 = h0 ^ 16;
    abo = AB_OFF + hf * 128 + cc * 32;
    abf = FLG_OFF + 8 + hf * 4;
    ov = (wq * 64 + rr) * 512 + hf * 64 + cc * 16;
    PIN(h0); PIN(h1); PIN(abo); PIN(abf); PIN(ov);
  };

  if (__builtin_expect(wid < 4, 1)) {
    const int lw = wq;
    __amdgpu_buffer_rsrc_t xr = __builtin_amdgcn_make_buffer_rsrc((void*)((const char*)x + xoff), 0, xlen, 0x00020000);
    __amdgpu_buffer_rsrc_t wr = __builtin_amdgcn_make_buffer_rsrc((void*)(w + cg * 64), 0, 768 * 512 - cg * 128, 0x00020000);
    const int rot = (cg & 1) * 2;
    LAS char* lbase = smem + lw * 1024;
    auto issue = [&](int u) {
      const int c = ((u >> 1) + rot) & 3, p = u & 1;
      LAS char* slot = lbase + (u % 3) * SLOT;
      if (p == 0) {
#pragma unroll
        for (int i = 0; i < 4; ++i)
          __builtin_amdgcn_raw_ptr_buffer_load_lds(wr, slot + 32768 + i * 4096, 16, wv, (i >> 1) * 262144 + c * 32768 + (i & 1) * 16384, 0, 0);
      } else {
#pragma unroll
        for (int i = 0; i < 2; ++i)
          __builtin_amdgcn_raw_ptr_buffer_load_lds(wr, slot + 32768 + i * 4096, 16, wv, 131072 + c * 32768 + i * 16384, 0, 0);
      }
#pragma unroll
      for (int i = 0; i < 8; ++i)
        { if (u < 4) __builtin_amdgcn_raw_ptr_buffer_load_lds(xr, slot + i * 4096, 16, xv, i * 32768 + p * 512 + c * 128, 0, XAUX0); else __builtin_amdgcn_raw_ptr_buffer_load_lds(xr, slot + i * 4096, 16, xv, i * 32768 + p * 512 + c * 128, 0, XAUX1); }
    };
    __builtin_amdgcn_raw_ptr_buffer_load_lds(wr, smem + PF_OFF + lw * 256, 4, pfv, 0, 0, 0);
    issue(0);
    STAMP(0);
    const __amdgpu_buffer_rsrc_t gr = __builtin_amdgcn_make_buffer_rsrc((void*)(ws + WS_GRAN + grp * 32768), 0, 32768, 0x00020000);
    const int ch = lw * 16 + (lane & 15);
#pragma unroll
    for (int k = 0; k < 8; ++k) {
      if (k + 1 < 8) {
        issue(k + 1);
        if ((k + 1) & 1) asm volatile("s_waitcnt vmcnt(10)" ::: "memory");
        else asm volatile("s_waitcnt vmcnt(12)" ::: "memory");
      } else {
        asm volatile("s_waitcnt vmcnt(0)" ::: "memory");
      }
      bar();
      if (k == 0) STAMP(1);
    }
    STAMP(2);
    u32 g_raw, b_raw, t0_, t1_; u32x2 tg;
    asm volatile("global_load_ushort %0, %5, %6\n\tglobal_load_ushort %1, %5, %7\n\t"
                 "global_load_dword %2, %8, %9\n\tglobal_load_dword %3, %10, %11\n\t"
                 "ds_read_b64 %4, %12\n\ts_waitcnt vmcnt(0) lgkmcnt(0)"
                 : "=&v"(g_raw), "=&v"(b_raw), "=&v"(t0_), "=&v"(t1_), "=&v"(tg)
                 : "v"((cg * 64 + ch) * 2), "s"(gam), "s"(bet), "v"(WS_GRAN - 128), "s"(ws), "v"((lane & 1) * (248 * 512)), "s"(outp), "v"(FLG_OFF + 32) : "memory");
    const u32 tagA = __builtin_amdgcn_readfirstlane(tg.x), tagB = __builtin_amdgcn_readfirstlane(tg.y);
    epi_addr();
    float g_c = bf2f(g_raw), b_c = bf2f(b_raw);
    int abw = AB_OFF + (((lane >> 4) & 1) * 64 + ch) * 4, abc = FLG_OFF + 8 + (lw >> 1) * 4, one = 1;
    u32 voff = (lane >> 4) * 8192 + ch * 16;
    int hfl = FLG_OFF + 16 + lw * 4;
    PIN(g_c); PIN(b_c); PIN(abw); PIN(voff); PIN(abc); PIN(one); PIN(hfl);
    float s = 0.f, q = 0.f;
    __builtin_amdgcn_s_setprio(GPRIO);
    lds_wait_ge(FLG_OFF + (lw >> 1) * 4, 4u);
    STAMP(3);
    if (lw < 2) __builtin_amdgcn_s_sleep(POLL_DELAY0); else __builtin_amdgcn_s_sleep(POLL_DELAY);
#pragma unroll 1
    for (u32 spin = 0; spin < (1u << 16); ++spin) {
      u32x4 gv[8];
      asm volatile(
          "buffer_load_dwordx4 %0, %8, %9, 0 offen sc1\n\t"
          "buffer_load_dwordx4 %1, %8, %9, 0 offen offset:1024 sc1\n\t"
          "buffer_load_dwordx4 %2, %8, %9, 0 offen offset:2048 sc1\n\t"
          "buffer_load_dwordx4 %3, %8, %9, 0 offen offset:3072 sc1\n\t"
          "buffer_load_dwordx4 %4, %8, %9, %10 offen sc1\n\t"
          "buffer_load_dwordx4 %5, %8, %9, %10 offen offset:1024 sc1\n\t"
          "buffer_load_dwordx4 %6, %8, %9, %10 offen offset:2048 sc1\n\t"
          "buffer_load_dwordx4 %7, %8, %9, %10 offen offset:3072 sc1\n\t"
          "s_waitcnt vmcnt(0)"
          : "=&v"(gv[0]), "=&v"(gv[1]), "=&v"(gv[2]), "=&v"(gv[3]), "=&v"(gv[4]), "=&v"(gv[5]), "=&v"(gv[6]), "=&v"(gv[7])
          : "v"(voff), "s"(gr), "s"(4096)
          : "memory");
      u32 m0 = voff & OOB, m1 = 0;
#pragma unroll
      for (int i = 0; i < 8; ++i) { m0 |= gv[i].y ^ tagA; m1 |= gv[i].w ^ tagB; }
      const float sn = ((__uint_as_float(gv[0].x) + __uint_as_float(gv[1].x)) + (__uint_as_float(gv[2].x) + __uint_as_float(gv[3].x))) +
                       ((__uint_as_float(gv[4].x) + __uint_as_float(gv[5].x)) + (__uint_as_float(gv[6].x) + __uint_as_float(gv[7].x)));
      const float qn = ((__uint_as_float(gv[0].z) + __uint_as_float(gv[1].z)) + (__uint_as_float(gv[2].z) + __uint_as_float(gv[3].z))) +
                       ((__uint_as_float(gv[4].z) + __uint_as_float(gv[5].z)) + (__uint_as_float(gv[6].z) + __uint_as_float(gv[7].z)));
      const bool ok = (m0 | m1) == 0u;
      s = ok ? sn : s; q = ok ? qn : q;
      voff = ok ? OOB : voff;
      if (__builtin_amdgcn_ballot_w64(voff != OOB) == 0ull) break;
      __builtin_amdgcn_s_sleep(POLL_SLEEP);
    }
    {
      auto r1 = __builtin_amdgcn_permlane32_swap(__float_as_uint(s), __float_as_uint(q), false, false);
      float u = __uint_as_float(r1[0]) + __uint_as_float(r1[1]);
      auto r2 = __builtin_amdgcn_permlane16_swap(__float_as_uint(u), __float_as_uint(u), false, false);
      u = __uint_as_float(r2[0]) + __uint_as_float(r2[1]);
      auto r3 = __builtin_amdgcn_permlane32_swap(__float_as_uint(u), __float_as_uint(u), false, false);
      s = __uint_as_float(r3[0]); q = __uint_as_float(r3[1]);
    }
    const float mu = s * (1.0f / L_);
    const float var = fmaxf(q * (1.0f / L_) - mu * mu, 0.f);
    const float a = g_c * __builtin_amdgcn_rsqf(var + 1e-5f);
    const float abv = (lane & 16) ? b_c - mu * a : a;
    asm volatile("ds_write_b32 %0, %1\n\ts_mov_b64 exec, 1\n\tds_add_u32 %2, %3\n\ts_mov_b64 exec, -1" :: "v"(abw), "v"(abv), "v"(abc), "v"(one) : "memory");
    STAMP(4);
    __builtin_amdgcn_s_setprio(0);
    if (t == 0 && lw == 0 && lane == 0)
      __hip_atomic_store((GAS u64*)(ws + WS_EPOCH + grp * 128), (u64)tagA | ((u64)tagB << 32), __ATOMIC_RELAXED, __HIP_MEMORY_SCOPE_AGENT);
#pragma unroll 1
    for (u32 sp = 0; sp < (1u << 22); ++sp) {
      u32 c;
      asm volatile("ds_read_b32 %0, %9\n\t"
                   "ds_read_b128 %1, %10\n\tds_read_b128 %2, %11\n\tds_read_b128 %3, %10 offset:4096\n\tds_read_b128 %4, %11 offset:4096\n\t"
                   "ds_read_b128 %5, %10 offset:8192\n\tds_read_b128 %6, %11 offset:8192\n\tds_read_b128 %7, %10 offset:12288\n\tds_read_b128 %8, %11 offset:12288\n\t"
                   "s_waitcnt lgkmcnt(0)"
                   : "=&v"(c), "=&v"(hv[0][0]), "=&v"(hv[0][1]), "=&v"(hv[1][0]), "=&v"(hv[1][1]), "=&v"(hv[2][0]), "=&v"(hv[2][1]), "=&v"(hv[3][0]), "=&v"(hv[3][1])
                   : "v"(hfl), "v"(h0), "v"(h1) : "memory");
      if (__builtin_amdgcn_readfirstlane(c) != 0) break;
      __builtin_amdgcn_s_sleep(2);
    }
  } else {
    STAMP(0);
    if (wq == 0) {
      u64 pc; int c0, e1, e2;
      asm volatile("s_getpc_b64 %0\n\t.Lpf_here:\n\ts_mov_b32 %1, .Lk_start-.Lpf_here\n\ts_mov_b32 %2, .Lkernel_end-.Lpf_here\n\ts_mov_b32 %3, .Lcons_end-.Lpf_here"
                   : "=s"(pc), "=s"(c0), "=s"(e1), "=s"(e2));
      const int co = c0 + ((bid >> 3) * 4 + (lane & 3)) * 128;
      __builtin_amdgcn_global_load_lds((const GAS u32*)((const char*)pc + (co < (e1 > e2 ? e1 : e2) ? co : 0)), (LAS u32*)(smem + PF_OFF + 1024), 4, 0, 0);
    }
    char* gp = ws + WS_GRAN + grp * 32768;
    asm volatile("" : "+s"(gp));
    const __amdgpu_buffer_rsrc_t gr = __builtin_amdgcn_make_buffer_rsrc((void*)gp, 0, 32768, 0x00020000);
    u32x2 ep;
    asm volatile("s_load_dwordx2 %0, %1, 0x0\n\ts_waitcnt lgkmcnt(0)" : "=s"(ep) : "s"(ws + WS_EPOCH + grp * 128) : "memory");
    u32 tagA = ep.x + 1u, tagB = ep.y + 1u;
    if (wq == 0 && lane < 10) *(LAS u32*)(smem + FLG_OFF + lane * 4) = lane < 8 ? 0u : (lane == 8 ? tagA : tagB);
    epi_addr();
    const int r = lane & 15, g = lane >> 4;
    const int rho = r < 4 ? 2 * r : (r >= 12 ? 2 * (r - 8) : 2 * (r - 4) + 1);
    int abase[2][2][2], abase3[2][2][2], bbase[2][4];
#pragma unroll
    for (int sh = 0; sh < 2; ++sh) {
      const int i = 64 * wq + rho + sh;
      const int i3 = (64 * wq + 48 + rho < nvalid) ? i + 48 : 254;
#pragma unroll
      for (int s = 0; s < 2; ++s) {
        abase[0][sh][s] = i * 128 + (((4 * s + g) ^ ((i >> 1) & 7)) << 4);
        abase3[0][sh][s] = i3 * 128 + (((4 * s + g) ^ ((i3 >> 1) & 7)) << 4);
        abase[1][sh][s] = abase[0][sh][s] + 2 * SLOT; abase3[1][sh][s] = abase3[0][sh][s] + 2 * SLOT;
        PIN(abase[0][sh][s]); PIN(abase3[0][sh][s]); PIN(abase[1][sh][s]); PIN(abase3[1][sh][s]);
      }
    }
    {
      const int q4 = (lane & 15) >> 2, p4 = lane & 3, sg = (q4 >> 1) + 2 * (g & 1);
#pragma unroll
      for (int n = 0; n < 4; ++n) {
        bbase[0][n] = 32768 + (8 * g + q4) * 128 + ((2 * (n ^ sg) + (p4 >> 1)) << 4) + 8 * (p4 & 1);
        bbase[1][n] = bbase[0][n] + 2 * SLOT;
        PIN(bbase[0][n]); PIN(bbase[1][n]);
      }
    }
    int hw0, hw1, redw = RED_OFF + wq * 512 + ((g >> 1) * 16 + r) * 8 + (g & 1) * 4, redo = RED_OFF + (lane & 31) * 8, flga = FLG_OFF, one = 1;
    u32 gvo = lane < 32 ? lane * 16 + t * 1024 : OOB, gvo1 = gvo + 512;
    int redo1 = redo + 256;
    PIN(gvo1); PIN(redo1);
    {
      const int rg = ((g & 1) ^ (g >> 1)) + 8 * (g >> 1);
      hw0 = (wq >> 1) * 2 * SLOT + (wq & 1) * 16384 + rg * 256 + r * 4;
      hw1 = hw0 ^ 16;
      PIN(hw0); PIN(hw1); PIN(redo); PIN(redw); PIN(gvo); PIN(flga); PIN(one);
    }
    PIN(tagA); PIN(tagB);
    f32x4 acc[4][4];
#pragma unroll
    for (int m = 0; m < 4; ++m)
#pragma unroll
      for (int n = 0; n < 4; ++n) acc[m][n] = f32x4{0.f, 0.f, 0.f, 0.f};
    auto rdA = [&](int gs, int m) {
      const int pos = gs % 6, u = 2 * (gs / 6) + (pos >= 4 ? 1 : 0), ts = (pos == 2 || pos == 3) ? 1 : 0, s = pos & 1;
      const int hi = (u % 3) == 2, so = (u % 3) == 1 ? SLOT : 0;
      return m < 3 ? *(const LAS bf16x8*)(uintptr_t)(abase[hi][ts][s] + so + m * 2048) : *(const LAS bf16x8*)(uintptr_t)(abase3[hi][ts][s] + so);
    };
    auto rdB = [&](int gs, int n, int tt) {
      const int pos = gs % 6, u = 2 * (gs / 6) + (pos >= 4 ? 1 : 0), ts = (pos == 2 || pos == 3) ? 1 : 0, s = pos & 1;
      const int hi = (u % 3) == 2, so = (u % 3) == 1 ? SLOT : 0;
      return __builtin_amdgcn_ds_read_tr16_b64_v4i16((LAS s16x4*)(uintptr_t)(bbase[hi][n] + so + ts * 8192 + s * 4096 + tt * 512));
    };
    auto rd = [&](Frags& f, int gs, int idx) {
      if (idx == 0) f.a[0] = rdA(gs, 0);
      else if (idx <= 8) f.b[(idx - 1) >> 1][(idx - 1) & 1] = rdB(gs, (idx - 1) >> 1, (idx - 1) & 1);
      else f.a[idx - 8] = rdA(gs, idx - 8);
    };
    Frags tf[6];
    auto rdT = [&](auto kc) __attribute__((always_inline)) {
      constexpr int k = decltype(kc)::value;
      constexpr int e = k < 32 ? k >> 3 : (k < 36 ? 4 : (k < 52 ? 5 + ((k - 36) >> 3) : 7 + ((k - 52) >> 2)));
      constexpr int i = k < 32 ? k & 7 : (k < 36 ? k - 32 : (k < 52 ? (k - 36) & 7 : (k - 52) & 3));
      constexpr int j = TJ(e);
      if constexpr (TH(e)) tf[j].b[2 + (i >> 1)][i & 1] = rdB(18 + j, 2 + (i >> 1), i & 1);
      else if constexpr (i == 0) tf[j].a[0] = rdA(18 + j, 0);
      else if constexpr (i <= 4) tf[j].b[(i - 1) >> 1][(i - 1) & 1] = rdB(18 + j, (i - 1) >> 1, (i - 1) & 1);
      else tf[j].a[i - 4] = rdA(18 + j, i - 4);
    };
    f32x2 s2[4], q2[4];
    auto stat = [&](int m, int n, int part) {
      const f32x2 v = part ? acc[m][n].zw : acc[m][n].xy;
      if (m == 0 && part == 0) { s2[n] = v; q2[n] = v * v; }
      else { s2[n] += v; q2[n] = __builtin_elementwise_fma(v, v, q2[n]); }
    };
    auto red_half = [&](int h) {
      auto xs = __builtin_amdgcn_permlane32_swap(__float_as_uint(s2[2 * h].x + s2[2 * h].y), __float_as_uint(s2[2 * h + 1].x + s2[2 * h + 1].y), false, false);
      auto xq = __builtin_amdgcn_permlane32_swap(__float_as_uint(q2[2 * h].x + q2[2 * h].y), __float_as_uint(q2[2 * h + 1].x + q2[2 * h + 1].y), false, false);
      const float us = __uint_as_float(xs[0]) + __uint_as_float(xs[1]), uq = __uint_as_float(xq[0]) + __uint_as_float(xq[1]);
      auto y = __builtin_amdgcn_permlane16_swap(__float_as_uint(us), __float_as_uint(uq), false, false);
      return __uint_as_float(y[0]) + __uint_as_float(y[1]);
    };
    Frags fr[2];
    __builtin_amdgcn_s_waitcnt(0x0070);
    asm volatile("" ::: "memory");
    __builtin_amdgcn_s_setprio(1);
    bar();
    STAMP(1);
#pragma unroll
    for (int i = 0; i < 12; ++i) { rd(fr[0], 0, i); SB(); }
#pragma unroll
    for (int gs = 0; gs < 17; ++gs) {
      Frags& cur = fr[gs & 1];
      Frags& nxt = fr[(gs + 1) & 1];
      const int np = (gs + 1) % 6;
      const bool newunit = np == 0 || np == 4;
      const int lead = newunit ? 4 : 1;
      WAIT_LGKM(3);
      SB();
#pragma unroll
      for (int i = 0; i < 16; ++i) {
        const int m = i >> 2, n = (m & 1) ? 3 - (i & 3) : (i & 3);
        if (i == 4 && !newunit) { WAIT_LGKM(4); SB(); }
        acc[m][n] = __builtin_amdgcn_mfma_f32_16x16x32_bf16(cur.a[m], cat(cur.b[n][0], cur.b[n][1]), acc[m][n], 0, 0, 0);
        SB();
        if (newunit && i == lead - 1) {
          WAIT_LGKM(0);
          asm volatile("" ::: "memory");
          bar();
          asm volatile("" ::: "memory");
          SB();
        }
        if (i >= lead - 1 && i < lead - 1 + 12) { rd(nxt, gs + 1, i - (lead - 1)); SB(); }
      }
    }
    WAIT_LGKM(3);
    SB();
    sfor<16>([&](auto ic) __attribute__((always_inline)) {
      constexpr int i = decltype(ic)::value, m = i >> 2, n = (m & 1) ? 3 - (i & 3) : (i & 3);
      acc[m][n] = __builtin_amdgcn_mfma_f32_16x16x32_bf16(fr[1].a[m], cat(fr[1].b[n][0], fr[1].b[n][1]), acc[m][n], 0, 0, 0);
      SB();
      if constexpr (i == 3) { WAIT_LGKM(0); asm volatile("" ::: "memory"); bar(); asm volatile("" ::: "memory"); SB(); }
      if constexpr (i >= 3) { rdT(CI(i - 3)); SB(); }
    });
    u32 cnt0; f32x4 ra, rb;
    sfor<96>([&](auto gc) __attribute__((always_inline)) {
      constexpr int gidx = decltype(gc)::value, e = gidx >> 3, i = gidx & 7, j = TJ(e), h = TH(e);
      constexpr int m = i >> 1, n = 2 * h + ((i ^ (i >> 1)) & 1);
      acc[m][n] = __builtin_amdgcn_mfma_f32_16x16x32_bf16(tf[j].a[m], cat(tf[j].b[n][0], tf[j].b[n][1]), acc[m][n], 0, 0, 0);
      SB();
      if constexpr (gidx < 23) { rdT(CI(13 + gidx)); SB(); }
      if constexpr (gidx == 24) { asm volatile("" ::: "memory"); bar(); asm volatile("" ::: "memory"); SB(); }
      if constexpr (gidx >= 24 && gidx < 60) { rdT(CI(36 + gidx - 24)); SB(); }
      if constexpr (gidx >= G_ST && gidx < G_ST + 8) { constexpr int k = gidx - G_ST; stat(k >> 1, (k ^ (k >> 1)) & 1, 0); stat(k >> 1, (k ^ (k >> 1)) & 1, 1); SB(); }
      if constexpr (gidx == G_ST + 9) {
        const float pr = red_half(0);
        asm volatile("ds_write_b32 %3, %4\n\t"
                     "s_mov_b64 exec, 1\n\t"
                     "ds_add_rtn_u32 %0, %5, %6\n\t"
                     "s_mov_b64 exec, -1\n\t"
                     "ds_read2st64_b64 %1, %7 offset1:1\n\t"
                     "ds_read2st64_b64 %2, %7 offset0:2 offset1:3"
                     : "=&v"(cnt0), "=&v"(ra), "=&v"(rb) : "v"(redw), "v"(pr), "v"(flga), "v"(one), "v"(redo) : "memory");
        SB();
      }
      if constexpr (gidx == G_ST + 9 + G_W) {
        asm volatile("s_waitcnt lgkmcnt(0)" : "+v"(cnt0), "+v"(ra), "+v"(rb) :: "memory");
        const f32x2 rr = (ra.xy + ra.zw) + (rb.xy + rb.zw);
        u32x4 gv = {__float_as_uint(rr.x), tagA, __float_as_uint(rr.y), tagB};
        __builtin_amdgcn_raw_buffer_store_b128(gv, gr, __builtin_amdgcn_readfirstlane(cnt0) == 3u ? gvo : OOB, 0, 16);
        SB();
        STAMP(2);
      }
      if constexpr (gidx >= 92) { constexpr int k = 2 * (gidx - 92); stat(k >> 2, 2 + (((k >> 1) ^ (k >> 2)) & 1), 0); stat(k >> 2, 2 + (((k >> 1) ^ (k >> 2)) & 1), 1); SB(); }
    });
#pragma unroll
    for (int k = 8; k < 16; ++k) stat(k >> 2, 2 + (((k >> 1) ^ (k >> 2)) & 1), k & 1);
    {
      const float pr = red_half(1);
      u32 old1, c0; f32x4 r01, r23;
      asm volatile("ds_write_b32 %4, %5 offset:256\n\t"
                   "s_mov_b64 exec, 1\n\t"
                   "ds_add_rtn_u32 %0, %6, %7 offset:4\n\t"
                   "s_mov_b64 exec, -1\n\t"
                   "ds_read2st64_b64 %1, %8 offset1:1\n\t"
                   "ds_read2st64_b64 %2, %8 offset0:2 offset1:3\n\t"
                   "ds_read_b32 %3, %6\n\t"
                   "s_waitcnt lgkmcnt(0)"
                   : "=&v"(old1), "=&v"(r01), "=&v"(r23), "=&v"(c0) : "v"(redw), "v"(pr), "v"(flga), "v"(one), "v"(redo1) : "memory");
      const f32x2 rr = (r01.xy + r01.zw) + (r23.xy + r23.zw);
      u32x4 gv = {__float_as_uint(rr.x), tagA, __float_as_uint(rr.y), tagB};
      __builtin_amdgcn_raw_buffer_store_b128(gv, gr, __builtin_amdgcn_readfirstlane(old1) == 3u ? gvo1 : OOB, 0, 16);
      __builtin_amdgcn_s_setprio(EPRIO);
      STAMP(3);
      if ((u32)__builtin_amdgcn_readfirstlane(c0) < 4u) lds_wait_ge(FLG_OFF, 4u);
    }
#pragma unroll
    for (int hh = 0; hh < 2; ++hh)
#pragma unroll
      for (int m = 0; m < 4; ++m)
#pragma unroll
        for (int j = 0; j < 4; ++j)
#pragma unroll
          for (int n = 2 * hh; n < 2 * hh + 2; ++n)
            *(LAS float*)(uintptr_t)(((j & 1) ? hw1 : hw0) + (16 * m + 2 * j) * 256 + (n ^ (j & 2)) * 64) = acc[m][n][j];
    asm volatile("ds_write_b32 %0, %1" :: "v"(FLG_OFF + 16 + wq * 4), "v"(one) : "memory");
#pragma unroll
    for (int i = 0; i < 4; ++i) { hv[i][0] = *(const LAS f32x4*)(uintptr_t)(h0 + i * 4096); hv[i][1] = *(const LAS f32x4*)(uintptr_t)(h1 + i * 4096); }
    STAMP(4);
    asm volatile(".Lcons_end:");
  }
  f32x4 av[2], bv[2];
#pragma unroll 1
  for (u32 sp = 0; sp < (1u << 22); ++sp) {
    u32 c;
    asm volatile("ds_read_b32 %0, %5\n\tds_read_b128 %1, %6\n\tds_read_b128 %2, %6 offset:16\n\tds_read_b128 %3, %6 offset:256\n\tds_read_b128 %4, %6 offset:272\n\ts_waitcnt lgkmcnt(0)"
                 : "=&v"(c), "=&v"(av[0]), "=&v"(av[1]), "=&v"(bv[0]), "=&v"(bv[1]) : "v"(abf), "v"(abo) : "memory");
    if ((u32)__builtin_amdgcn_readfirstlane(c) >= 2u) break;
  }
  STAMP(5);
  const __amdgpu_buffer_rsrc_t orr = __builtin_amdgcn_make_buffer_rsrc((void*)outp, 0, (int)(xlen >> 1) - 640, 0x00020000);
  const float K1 = -2.0f * 0.7978845608028654f * 1.4426950408889634f;
  const float K3 = K1 * 0.044715f;
  const f32x2 K1v = {K1, K1}, K3v = {K3, K3}, ONE = {1.f, 1.f};
#pragma unroll
  for (int i = 0; i < 4; ++i) {
    f32x2 z[4], e[4];
#pragma unroll
    for (int j = 0; j < 4; ++j) {
      const f32x2 h2 = (j & 1) ? hv[i][j >> 1].zw : hv[i][j >> 1].xy;
      const f32x2 a2 = (j & 1) ? av[j >> 1].zw : av[j >> 1].xy;
      const f32x2 b2 = (j & 1) ? bv[j >> 1].zw : bv[j >> 1].xy;
      z[j] = __builtin_elementwise_fma(h2, a2, b2);
      e[j] = z[j] * __builtin_elementwise_fma(z[j] * z[j], K3v, K1v);
    }
#pragma unroll
    for (int j = 0; j < 4; ++j) { e[j].x = __builtin_amdgcn_exp2f(e[j].x); e[j].y = __builtin_amdgcn_exp2f(e[j].y); }
#pragma unroll
    for (int j = 0; j < 4; ++j) e[j] += ONE;
#pragma unroll
    for (int j = 0; j < 4; ++j) { e[j].x = __builtin_amdgcn_rcpf(e[j].x); e[j].y = __builtin_amdgcn_rcpf(e[j].y); }
    u32x4 o;
#pragma unroll
    for (int j = 0; j < 4; ++j) {
      const f32x2 y = z[j] * e[j];
      bf16x2 pk = {(__bf16)y.x, (__bf16)y.y};
      o[j] = __builtin_bit_cast(u32, pk);
    }
    __builtin_amdgcn_raw_buffer_store_b128(o, orr, ov + i * 16 * 512, 0, OUT_AUX);
  }
  STAMP(6);
  STAMP(7);
  asm volatile(".Lkernel_end:");
}

extern "C" void kernel_launch(void* const* d_in, const int* in_sizes, int n_in, void* d_out, int out_size,
                              void* d_ws, size_t ws_size, hipStream_t stream) {
  (void)in_sizes; (void)n_in; (void)out_size; (void)ws_size;
  (void)hipFuncSetAttribute((const void*)conv_stem_kernel, hipFuncAttributeMaxDynamicSharedMemorySize, LDS_BYTES);
  hipLaunchKernelGGL(conv_stem_kernel, dim3(256), dim3(512), LDS_BYTES, stream, (const u16*)d_in[0], (const u16*)d_in[1],
                     (const u16*)d_in[2], (const u16*)d_in[3], (u16*)d_out, (char*)d_ws
  );
}
```

```cpp
#include <hip/hip_runtime.h>
#include <hip/hip_bf16.h>
#include <utility>

typedef unsigned short u16;
typedef unsigned int u32;
typedef unsigned long long u64;
typedef __attribute__((ext_vector_type(4))) short s16x4;
typedef __attribute__((ext_vector_type(8))) __bf16 bf16x8;
typedef __attribute__((ext_vector_type(2))) __bf16 bf16x2;
typedef __attribute__((ext_vector_type(4))) float f32x4;
typedef __attribute__((ext_vector_type(2))) float f32x2;
typedef __attribute__((ext_vector_type(4))) u32 u32x4;
typedef __attribute__((ext_vector_type(2))) u32 u32x2;
#define LAS __attribute__((address_space(3)))
#define GAS __attribute__((address_space(1)))

constexpr int T_ = 16000, L_ = 7999;
constexpr int TR = 250;
constexpr int SLOT = 48 * 1024;
constexpr int RED_OFF = 3 * SLOT;
constexpr int AB_OFF = RED_OFF + 2048;
constexpr int FLG_OFF = AB_OFF + 512;
constexpr int PF_OFF = FLG_OFF + 256;
constexpr int LDS_BYTES = PF_OFF + 1280;
constexpr int WS_EPOCH = 0;
constexpr int WS_GRAN = 4096;
constexpr u32 OOB = 0x40000000u;
#define POLL_DELAY 16
#define OUT_AUX 16
#define POLL_DELAY0 20
#define EPRIO 0
#define G_ST 52
#define G_W 7
#define GPRIO 0
#define POLL_SLEEP 1
#define PREWRITE 0

#define STAMP(i) do {} while (0)
#define SB() __builtin_amdgcn_sched_barrier(0)
#define PIN(v) asm volatile("" : "+v"(v))
#define WAIT_LGKM(n) __builtin_amdgcn_s_waitcnt(0xC07F | ((n) << 8))
__device__ __forceinline__ void bar() { __builtin_amdgcn_s_barrier(); }
__device__ __forceinline__ float bf2f(u32 v) { return __uint_as_float(v << 16); }
__device__ __forceinline__ bf16x8 cat(s16x4 a, s16x4 b) { return __builtin_bit_cast(bf16x8, __builtin_shufflevector(a, b, 0, 1, 2, 3, 4, 5, 6, 7)); }
__device__ __forceinline__ void lds_wait_ge(int a, u32 v) {
#pragma unroll 1
  for (u32 sp = 0; sp < (1u << 22); ++sp) {
    u32 c; asm volatile("ds_read_b32 %0, %1\n\ts_waitcnt lgkmcnt(0)" : "=v"(c) : "v"(a) : "memory");
    if ((u32)__builtin_amdgcn_readfirstlane(c) >= v) break;
    __builtin_amdgcn_s_sleep(1);
  }
}

#define TJ(e) ((e) < 4 ? (e) : ((e) == 4 ? 0 : ((e) < 7 ? (e) - 1 : (e) - 6)))
#define TH(e) ((e) == 4 || (e) >= 7 ? 1 : 0)
template <int... I, class F> __device__ __forceinline__ void sfor_(std::integer_sequence<int, I...>, F&& f) { (f(std::integral_constant<int, I>{}), ...); }
template <int N, class F> __device__ __forceinline__ void sfor(F&& f) { sfor_(std::make_integer_sequence<int, N>{}, f); }
#define CI(x) std::integral_constant<int, (x)>{}
struct Frags { bf16x8 a[4]; s16x4 b[4][2]; };

__global__ __launch_bounds__(512, 2) void conv_stem_kernel(const u16* __restrict__ x, const u16* __restrict__ w,
                                                           const u16* __restrict__ gam, const u16* __restrict__ bet,
                                                           u16* __restrict__ out, char* __restrict__ ws
) {
  extern __shared__ __attribute__((aligned(16))) char smem_[];
  LAS char* smem = (LAS char*)smem_;
  asm volatile(".Lk_start:");
  const int bid = blockIdx.x;
  const int cg = (bid >> 3) & 3;
  const int rt = (bid & 7) * 8 + (bid >> 5);
  const int b = rt >> 5, t = rt & 31;
  const int l0 = t * TR;
  const int nvalid = (L_ - l0) < TR ? (L_ - l0) : TR;
  const int grp = b * 4 + cg;
  const int wid = __builtin_amdgcn_readfirstlane(threadIdx.x >> 6);
  const int lane = threadIdx.x & 63;
  const int wq = wid & 3;
  int h0, h1, abo, abf, ov;
  f32x4 hv[4][2];
  int xv = wq * 8192 + (lane >> 3) * 1024 + (((lane & 7) ^ (lane >> 4) ^ ((wq & 1) << 2)) << 4);
  int wv = wq * 4096 + (lane >> 3) * 512 + (((lane & 7) ^ (((lane >> 4) & 1) << 1) ^ ((wq & 1) << 2)) << 4);
  u32 xoff = (u32)(b * T_ + 2 * l0) * 512u, xlen = (u32)(2 * nvalid + 1) * 512u;
  const int pfi = (bid >> 5) * 48 + wq * 12 + lane;
  int pfv = lane < 12 ? ((pfi >> 7) * 256 + ((cg & 1) ^ 1) * 128 + (pfi & 127)) * 512 : (int)OOB;
  PIN(xv); PIN(wv); PIN(pfv);
  asm volatile("" : "+s"(xoff), "+s"(xlen));
  SB();
  asm volatile("" :: "s"(x), "s"(w), "s"(gam), "s"(bet), "s"(out), "s"(ws));
  u16* const outp = out + ((size_t)(b * L_ + l0) * 256 + cg * 64);
  auto epi_addr = [&]() {
    const int hf = wid < 4 ? 1 : 0;
    const int rr = lane >> 2, cc = lane & 3, sw = ((rr >> 1) & 1) | (((rr >> 2) & 1) << 3);
    const int hb = (wq >> 1) * 2 * SLOT + (wq & 1) * 16384 + rr * 256;
    h0 = hb + (((8 * hf + 2 * cc) ^ sw) << 4); h1 = h0 ^ 16;
    abo = AB_OFF + hf * 128 + cc * 32;
    abf = FLG_OFF + 8 + hf * 4;
    ov = (wq * 64 + rr) * 512 + hf * 64 + cc * 16;
    PIN(h0); PIN(h1); PIN(abo); PIN(abf); PIN(ov);
  };

  if (__builtin_expect(wid < 4, 1)) {
    const int lw = wq;
    __amdgpu_buffer_rsrc_t xr = __builtin_amdgcn_make_buffer_rsrc((void*)((const char*)x + xoff), 0, xlen, 0x00020000);
    __amdgpu_buffer_rsrc_t wr = __builtin_amdgcn_make_buffer_rsrc((void*)(w + cg * 64), 0, 768 * 512 - cg * 128, 0x00020000);
    const int rot = (cg & 1) * 2;
    LAS char* lbase = smem + lw * 1024;
    auto issue = [&](int u) {
      const int c = ((u >> 1) + rot) & 3, p = u & 1;
      LAS char* slot = lbase + (u % 3) * SLOT;
      if (p == 0) {
#pragma unroll
        for (int i = 0; i < 4; ++i)
          __builtin_amdgcn_raw_ptr_buffer_load_lds(wr, slot + 32768 + i * 4096, 16, wv, (i >> 1) * 262144 + c * 32768 + (i & 1) * 16384, 0, 0);
      } else {
#pragma unroll
        for (int i = 0; i < 2; ++i)
          __builtin_amdgcn_raw_ptr_buffer_load_lds(wr, slot + 32768 + i * 4096, 16, wv, 131072 + c * 32768 + i * 16384, 0, 0);
      }
#pragma unroll
      for (int i = 0; i < 8; ++i)
        __builtin_amdgcn_raw_ptr_buffer_load_lds(xr, slot + i * 4096, 16, xv, i * 32768 + p * 512 + c * 128, 0, 0);
    };
    issue(0);
    STAMP(0);
    const __amdgpu_buffer_rsrc_t gr = __builtin_amdgcn_make_buffer_rsrc((void*)(ws + WS_GRAN + grp * 32768), 0, 32768, 0x00020000);
    const int ch = lw * 16 + (lane & 15);
#pragma unroll
    for (int k = 0; k < 8; ++k) {
      if (k + 1 < 8) {
        issue(k + 1);
        if ((k + 1) & 1) asm volatile("s_waitcnt vmcnt(10)" ::: "memory");
        else asm volatile("s_waitcnt vmcnt(12)" ::: "memory");
      } else {
        asm volatile("s_waitcnt vmcnt(0)" ::: "memory");
      }
      bar();
      if (k == 0) STAMP(1);
      if (k == 0) __builtin_amdgcn_raw_ptr_buffer_load_lds(wr, smem + PF_OFF + lw * 256, 4, pfv, 0, 0, 0);
    }
    STAMP(2);
    u32 g_raw, b_raw, t0_, t1_; u32x2 tg;
    asm volatile("global_load_ushort %0, %5, %6\n\tglobal_load_ushort %1, %5, %7\n\t"
                 "global_load_dword %2, %8, %9\n\tglobal_load_dword %3, %10, %11\n\t"
                 "ds_read_b64 %4, %12\n\ts_waitcnt vmcnt(0) lgkmcnt(0)"
                 : "=&v"(g_raw), "=&v"(b_raw), "=&v"(t0_), "=&v"(t1_), "=&v"(tg)
                 : "v"((cg * 64 + ch) * 2), "s"(gam), "s"(bet), "v"(WS_GRAN - 128), "s"(ws), "v"((lane & 1) * (248 * 512)), "s"(outp), "v"(FLG_OFF + 32) : "memory");
    const u32 tagA = __builtin_amdgcn_readfirstlane(tg.x), tagB = __builtin_amdgcn_readfirstlane(tg.y);
    epi_addr();
    float g_c = bf2f(g_raw), b_c = bf2f(b_raw);
    int abw = AB_OFF + (((lane >> 4) & 1) * 64 + ch) * 4, abc = FLG_OFF + 8 + (lw >> 1) * 4, one = 1;
    u32 voff = (lane >> 4) * 8192 + ch * 16;
    int hfl = FLG_OFF + 16 + lw * 4;
    PIN(g_c); PIN(b_c); PIN(abw); PIN(voff); PIN(abc); PIN(one); PIN(hfl);
    float s = 0.f, q = 0.f;
    __builtin_amdgcn_s_setprio(GPRIO);
    lds_wait_ge(FLG_OFF + (lw >> 1) * 4, 4u);
    STAMP(3);
    if (lw < 2) __builtin_amdgcn_s_sleep(POLL_DELAY0); else __builtin_amdgcn_s_sleep(POLL_DELAY);
#pragma unroll 1
    for (u32 spin = 0; spin < (1u << 16); ++spin) {
      u32x4 gv[8];
      asm volatile(
          "buffer_load_dwordx4 %0, %8, %9, 0 offen sc1\n\t"
          "buffer_load_dwordx4 %1, %8, %9, 0 offen offset:1024 sc1\n\t"
          "buffer_load_dwordx4 %2, %8, %9, 0 offen offset:2048 sc1\n\t"
          "buffer_load_dwordx4 %3, %8, %9, 0 offen offset:3072 sc1\n\t"
          "buffer_load_dwordx4 %4, %8, %9, %10 offen sc1\n\t"
          "buffer_load_dwordx4 %5, %8, %9, %10 offen offset:1024 sc1\n\t"
          "buffer_load_dwordx4 %6, %8, %9, %10 offen offset:2048 sc1\n\t"
          "buffer_load_dwordx4 %7, %8, %9, %10 offen offset:3072 sc1\n\t"
          "s_waitcnt vmcnt(0)"
          : "=&v"(gv[0]), "=&v"(gv[1]), "=&v"(gv[2]), "=&v"(gv[3]), "=&v"(gv[4]), "=&v"(gv[5]), "=&v"(gv[6]), "=&v"(gv[7])
          : "v"(voff), "s"(gr), "s"(4096)
          : "memory");
      u32 m0 = voff & OOB, m1 = 0;
#pragma unroll
      for (int i = 0; i < 8; ++i) { m0 |= gv[i].y ^ tagA; m1 |= gv[i].w ^ tagB; }
      const float sn = ((__uint_as_float(gv[0].x) + __uint_as_float(gv[1].x)) + (__uint_as_float(gv[2].x) + __uint_as_float(gv[3].x))) +
                       ((__uint_as_float(gv[4].x) + __uint_as_float(gv[5].x)) + (__uint_as_float(gv[6].x) + __uint_as_float(gv[7].x)));
      const float qn = ((__uint_as_float(gv[0].z) + __uint_as_float(gv[1].z)) + (__uint_as_float(gv[2].z) + __uint_as_float(gv[3].z))) +
                       ((__uint_as_float(gv[4].z) + __uint_as_float(gv[5].z)) + (__uint_as_float(gv[6].z) + __uint_as_float(gv[7].z)));
      const bool ok = (m0 | m1) == 0u;
      s = ok ? sn : s; q = ok ? qn : q;
      voff = ok ? OOB : voff;
      if (__builtin_amdgcn_ballot_w64(voff != OOB) == 0ull) break;
      __builtin_amdgcn_s_sleep(POLL_SLEEP);
    }
    {
      auto r1 = __builtin_amdgcn_permlane32_swap(__float_as_uint(s), __float_as_uint(q), false, false);
      float u = __uint_as_float(r1[0]) + __uint_as_float(r1[1]);
      auto r2 = __builtin_amdgcn_permlane16_swap(__float_as_uint(u), __float_as_uint(u), false, false);
      u = __uint_as_float(r2[0]) + __uint_as_float(r2[1]);
      auto r3 = __builtin_amdgcn_permlane32_swap(__float_as_uint(u), __float_as_uint(u), false, false);
      s = __uint_as_float(r3[0]); q = __uint_as_float(r3[1]);
    }
    const float mu = s * (1.0f / L_);
    const float var = fmaxf(q * (1.0f / L_) - mu * mu, 0.f);
    const float a = g_c * __builtin_amdgcn_rsqf(var + 1e-5f);
    const float abv = (lane & 16) ? b_c - mu * a : a;
    asm volatile("ds_write_b32 %0, %1\n\ts_mov_b64 exec, 1\n\tds_add_u32 %2, %3\n\ts_mov_b64 exec, -1" :: "v"(abw), "v"(abv), "v"(abc), "v"(one) : "memory");
    STAMP(4);
    __builtin_amdgcn_s_setprio(0);
    if (t == 0 && lw == 0 && lane == 0)
      __hip_atomic_store((GAS u64*)(ws + WS_EPOCH + grp * 128), (u64)tagA | ((u64)tagB << 32), __ATOMIC_RELAXED, __HIP_MEMORY_SCOPE_AGENT);
#pragma unroll 1
    for (u32 sp = 0; sp < (1u << 22); ++sp) {
      u32 c;
      asm volatile("ds_read_b32 %0, %9\n\t"
                   "ds_read_b128 %1, %10\n\tds_read_b128 %2, %11\n\tds_read_b128 %3, %10 offset:4096\n\tds_read_b128 %4, %11 offset:4096\n\t"
                   "ds_read_b128 %5, %10 offset:8192\n\tds_read_b128 %6, %11 offset:8192\n\tds_read_b128 %7, %10 offset:12288\n\tds_read_b128 %8, %11 offset:12288\n\t"
                   "s_waitcnt lgkmcnt(0)"
                   : "=&v"(c), "=&v"(hv[0][0]), "=&v"(hv[0][1]), "=&v"(hv[1][0]), "=&v"(hv[1][1]), "=&v"(hv[2][0]), "=&v"(hv[2][1]), "=&v"(hv[3][0]), "=&v"(hv[3][1])
                   : "v"(hfl), "v"(h0), "v"(h1) : "memory");
      if (__builtin_amdgcn_readfirstlane(c) != 0) break;
      __builtin_amdgcn_s_sleep(2);
    }
  } else {
    STAMP(0);
    if (wq == 0) {
      u64 pc; int c0, e1, e2;
      asm volatile("s_getpc_b64 %0\n\t.Lpf_here:\n\ts_mov_b32 %1, .Lk_start-.Lpf_here\n\ts_mov_b32 %2, .Lkernel_end-.Lpf_here\n\ts_mov_b32 %3, .Lcons_end-.Lpf_here"
                   : "=s"(pc), "=s"(c0), "=s"(e1), "=s"(e2));
      const int co = c0 + ((bid >> 3) * 4 + (lane & 3)) * 128;
      __builtin_amdgcn_global_load_lds((const GAS u32*)((const char*)pc + (co < (e1 > e2 ? e1 : e2) ? co : 0)), (LAS u32*)(smem + PF_OFF + 1024), 4, 0, 0);
    }
    char* gp = ws + WS_GRAN + grp * 32768;
    asm volatile("" : "+s"(gp));
    const __amdgpu_buffer_rsrc_t gr = __builtin_amdgcn_make_buffer_rsrc((void*)gp, 0, 32768, 0x00020000);
    u32x2 ep;
    asm volatile("s_load_dwordx2 %0, %1, 0x0\n\ts_waitcnt lgkmcnt(0)" : "=s"(ep) : "s"(ws + WS_EPOCH + grp * 128) : "memory");
    u32 tagA = ep.x + 1u, tagB = ep.y + 1u;
    if (wq == 0 && lane < 10) *(LAS u32*)(smem + FLG_OFF + lane * 4) = lane < 8 ? 0u : (lane == 8 ? tagA : tagB);
    epi_addr();
    const int r = lane & 15, g = lane >> 4;
    const int rho = r < 4 ? 2 * r : (r >= 12 ? 2 * (r - 8) : 2 * (r - 4) + 1);
    int abase[2][2][2], abase3[2][2][2], bbase[2][4];
#pragma unroll
    for (int sh = 0; sh < 2; ++sh) {
      const int i = 64 * wq + rho + sh;
      const int i3 = (64 * wq + 48 + rho < nvalid) ? i + 48 : 254;
#pragma unroll
      for (int s = 0; s < 2; ++s) {
        abase[0][sh][s] = i * 128 + (((4 * s + g) ^ ((i >> 1) & 7)) << 4);
        abase3[0][sh][s] = i3 * 128 + (((4 * s + g) ^ ((i3 >> 1) & 7)) << 4);
        abase[1][sh][s] = abase[0][sh][s] + 2 * SLOT; abase3[1][sh][s] = abase3[0][sh][s] + 2 * SLOT;
        PIN(abase[0][sh][s]); PIN(abase3[0][sh][s]); PIN(abase[1][sh][s]); PIN(abase3[1][sh][s]);
      }
    }
    {
      const int q4 = (lane & 15) >> 2, p4 = lane & 3, sg = (q4 >> 1) + 2 * (g & 1);
#pragma unroll
      for (int n = 0; n < 4; ++n) {
        bbase[0][n] = 32768 + (8 * g + q4) * 128 + ((2 * (n ^ sg) + (p4 >> 1)) << 4) + 8 * (p4 & 1);
        bbase[1][n] = bbase[0][n] + 2 * SLOT;
        PIN(bbase[0][n]); PIN(bbase[1][n]);
      }
    }
    int hw0, hw1, redw = RED_OFF + wq * 512 + ((g >> 1) * 16 + r) * 8 + (g & 1) * 4, redo = RED_OFF + (lane & 31) * 8, flga = FLG_OFF, one = 1;
    u32 gvo = lane < 32 ? lane * 16 + t * 1024 : OOB, gvo1 = gvo + 512;
    int redo1 = redo + 256;
    PIN(gvo1); PIN(redo1);
    {
      const int rg = ((g & 1) ^ (g >> 1)) + 8 * (g >> 1);
      hw0 = (wq >> 1) * 2 * SLOT + (wq & 1) * 16384 + rg * 256 + r * 4;
      hw1 = hw0 ^ 16;
      PIN(hw0); PIN(hw1); PIN(redo); PIN(redw); PIN(gvo); PIN(flga); PIN(one);
    }
    PIN(tagA); PIN(tagB);
    f32x4 acc[4][4];
#pragma unroll
    for (int m = 0; m < 4; ++m)
#pragma unroll
      for (int n = 0; n < 4; ++n) acc[m][n] = f32x4{0.f, 0.f, 0.f, 0.f};
    auto rdA = [&](int gs, int m) {
      const int pos = gs % 6, u = 2 * (gs / 6) + (pos >= 4 ? 1 : 0), ts = (pos == 2 || pos == 3) ? 1 : 0, s = pos & 1;
      const int hi = (u % 3) == 2, so = (u % 3) == 1 ? SLOT : 0;
      return m < 3 ? *(const LAS bf16x8*)(uintptr_t)(abase[hi][ts][s] + so + m * 2048) : *(const LAS bf16x8*)(uintptr_t)(abase3[hi][ts][s] + so);
    };
    auto rdB = [&](int gs, int n, int tt) {
      const int pos = gs % 6, u = 2 * (gs / 6) + (pos >= 4 ? 1 : 0), ts = (pos == 2 || pos == 3) ? 1 : 0, s = pos & 1;
      const int hi = (u % 3) == 2, so = (u % 3) == 1 ? SLOT : 0;
      return __builtin_amdgcn_ds_read_tr16_b64_v4i16((LAS s16x4*)(uintptr_t)(bbase[hi][n] + so + ts * 8192 + s * 4096 + tt * 512));
    };
    auto rd = [&](Frags& f, int gs, int idx) {
      if (idx == 0) f.a[0] = rdA(gs, 0);
      else if (idx <= 8) f.b[(idx - 1) >> 1][(idx - 1) & 1] = rdB(gs, (idx - 1) >> 1, (idx - 1) & 1);
      else f.a[idx - 8] = rdA(gs, idx - 8);
    };
    Frags tf[6];
    auto rdT = [&](auto kc) __attribute__((always_inline)) {
      constexpr int k = decltype(kc)::value;
      constexpr int e = k < 32 ? k >> 3 : (k < 36 ? 4 : (k < 52 ? 5 + ((k - 36) >> 3) : 7 + ((k - 52) >> 2)));
      constexpr int i = k < 32 ? k & 7 : (k < 36 ? k - 32 : (k < 52 ? (k - 36) & 7 : (k - 52) & 3));
      constexpr int j = TJ(e);
      if constexpr (TH(e)) tf[j].b[2 + (i >> 1)][i & 1] = rdB(18 + j, 2 + (i >> 1), i & 1);
      else if constexpr (i == 0) tf[j].a[0] = rdA(18 + j, 0);
      else if constexpr (i <= 4) tf[j].b[(i - 1) >> 1][(i - 1) & 1] = rdB(18 + j, (i - 1) >> 1, (i - 1) & 1);
      else tf[j].a[i - 4] = rdA(18 + j, i - 4);
    };
    f32x2 s2[4], q2[4];
    auto stat = [&](int m, int n, int part) {
      const f32x2 v = part ? acc[m][n].zw : acc[m][n].xy;
      if (m == 0 && part == 0) { s2[n] = v; q2[n] = v * v; }
      else { s2[n] += v; q2[n] = __builtin_elementwise_fma(v, v, q2[n]); }
    };
    auto red_half = [&](int h) {
      auto xs = __builtin_amdgcn_permlane32_swap(__float_as_uint(s2[2 * h].x + s2[2 * h].y), __float_as_uint(s2[2 * h + 1].x + s2[2 * h + 1].y), false, false);
      auto xq = __builtin_amdgcn_permlane32_swap(__float_as_uint(q2[2 * h].x + q2[2 * h].y), __float_as_uint(q2[2 * h + 1].x + q2[2 * h + 1].y), false, false);
      const float us = __uint_as_float(xs[0]) + __uint_as_float(xs[1]), uq = __uint_as_float(xq[0]) + __uint_as_float(xq[1]);
      auto y = __builtin_amdgcn_permlane16_swap(__float_as_uint(us), __float_as_uint(uq), false, false);
      return __uint_as_float(y[0]) + __uint_as_float(y[1]);
    };
    Frags fr[2];
    __builtin_amdgcn_s_waitcnt(0x0070);
    asm volatile("" ::: "memory");
    __builtin_amdgcn_s_setprio(1);
    bar();
    STAMP(1);
#pragma unroll
    for (int i = 0; i < 12; ++i) { rd(fr[0], 0, i); SB(); }
#pragma unroll
    for (int gs = 0; gs < 17; ++gs) {
      Frags& cur = fr[gs & 1];
      Frags& nxt = fr[(gs + 1) & 1];
      const int np = (gs + 1) % 6;
      const bool newunit = np == 0 || np == 4;
      const int lead = newunit ? 4 : 1;
      WAIT_LGKM(3);
      SB();
#pragma unroll
      for (int i = 0; i < 16; ++i) {
        const int m = i >> 2, n = (m & 1) ? 3 - (i & 3) : (i & 3);
        if (i == 4 && !newunit) { WAIT_LGKM(4); SB(); }
        acc[m][n] = __builtin_amdgcn_mfma_f32_16x16x32_bf16(cur.a[m], cat(cur.b[n][0], cur.b[n][1]), acc[m][n], 0, 0, 0);
        SB();
        if (newunit && i == lead - 1) {
          WAIT_LGKM(0);
          asm volatile("" ::: "memory");
          bar();
          asm volatile("" ::: "memory");
          SB();
        }
        if (i >= lead - 1 && i < lead - 1 + 12) { rd(nxt, gs + 1, i - (lead - 1)); SB(); }
      }
    }
    WAIT_LGKM(3);
    SB();
    sfor<16>([&](auto ic) __attribute__((always_inline)) {
      constexpr int i = decltype(ic)::value, m = i >> 2, n = (m & 1) ? 3 - (i & 3) : (i & 3);
      acc[m][n] = __builtin_amdgcn_mfma_f32_16x16x32_bf16(fr[1].a[m], cat(fr[1].b[n][0], fr[1].b[n][1]), acc[m][n], 0, 0, 0);
      SB();
      if constexpr (i == 3) { WAIT_LGKM(0); asm volatile("" ::: "memory"); bar(); asm volatile("" ::: "memory"); SB(); }
      if constexpr (i >= 3) { rdT(CI(i - 3)); SB(); }
    });
    u32 cnt0; f32x4 ra, rb;
    sfor<96>([&](auto gc) __attribute__((always_inline)) {
      constexpr int gidx = decltype(gc)::value, e = gidx >> 3, i = gidx & 7, j = TJ(e), h = TH(e);
      constexpr int m = i >> 1, n = 2 * h + ((i ^ (i >> 1)) & 1);
      acc[m][n] = __builtin_amdgcn_mfma_f32_16x16x32_bf16(tf[j].a[m], cat(tf[j].b[n][0], tf[j].b[n][1]), acc[m][n], 0, 0, 0);
      SB();
      if constexpr (gidx < 23) { rdT(CI(13 + gidx)); SB(); }
      if constexpr (gidx == 24) { asm volatile("" ::: "memory"); bar(); asm volatile("" ::: "memory"); SB(); }
      if constexpr (gidx >= 24 && gidx < 60) { rdT(CI(36 + gidx - 24)); SB(); }
      if constexpr (gidx >= G_ST && gidx < G_ST + 8) { constexpr int k = gidx - G_ST; stat(k >> 1, (k ^ (k >> 1)) & 1, 0); stat(k >> 1, (k ^ (k >> 1)) & 1, 1); SB(); }
      if constexpr (gidx == G_ST + 9) {
        const float pr = red_half(0);
        asm volatile("ds_write_b32 %3, %4\n\t"
                     "s_mov_b64 exec, 1\n\t"
                     "ds_add_rtn_u32 %0, %5, %6\n\t"
                     "s_mov_b64 exec, -1\n\t"
                     "ds_read2st64_b64 %1, %7 offset1:1\n\t"
                     "ds_read2st64_b64 %2, %7 offset0:2 offset1:3"
                     : "=&v"(cnt0), "=&v"(ra), "=&v"(rb) : "v"(redw), "v"(pr), "v"(flga), "v"(one), "v"(redo) : "memory");
        SB();
      }
      if constexpr (gidx == G_ST + 9 + G_W) {
        asm volatile("s_waitcnt lgkmcnt(0)" : "+v"(cnt0), "+v"(ra), "+v"(rb) :: "memory");
        const f32x2 rr = (ra.xy + ra.zw) + (rb.xy + rb.zw);
        u32x4 gv = {__float_as_uint(rr.x), tagA, __float_as_uint(rr.y), tagB};
        __builtin_amdgcn_raw_buffer_store_b128(gv, gr, __builtin_amdgcn_readfirstlane(cnt0) == 3u ? gvo : OOB, 0, 16);
        SB();
        STAMP(2);
      }
      if constexpr (gidx >= 92) { constexpr int k = 2 * (gidx - 92); stat(k >> 2, 2 + (((k >> 1) ^ (k >> 2)) & 1), 0); stat(k >> 2, 2 + (((k >> 1) ^ (k >> 2)) & 1), 1); SB(); }
    });
#pragma unroll
    for (int k = 8; k < 16; ++k) stat(k >> 2, 2 + (((k >> 1) ^ (k >> 2)) & 1), k & 1);
    {
      const float pr = red_half(1);
      u32 old1, c0; f32x4 r01, r23;
      asm volatile("ds_write_b32 %4, %5 offset:256\n\t"
                   "s_mov_b64 exec, 1\n\t"
                   "ds_add_rtn_u32 %0, %6, %7 offset:4\n\t"
                   "s_mov_b64 exec, -1\n\t"
                   "ds_read2st64_b64 %1, %8 offset1:1\n\t"
                   "ds_read2st64_b64 %2, %8 offset0:2 offset1:3\n\t"
                   "ds_read_b32 %3, %6\n\t"
                   "s_waitcnt lgkmcnt(0)"
                   : "=&v"(old1), "=&v"(r01), "=&v"(r23), "=&v"(c0) : "v"(redw), "v"(pr), "v"(flga), "v"(one), "v"(redo1) : "memory");
      const f32x2 rr = (r01.xy + r01.zw) + (r23.xy + r23.zw);
      u32x4 gv = {__float_as_uint(rr.x), tagA, __float_as_uint(rr.y), tagB};
      __builtin_amdgcn_raw_buffer_store_b128(gv, gr, __builtin_amdgcn_readfirstlane(old1) == 3u ? gvo1 : OOB, 0, 16);
      __builtin_amdgcn_s_setprio(EPRIO);
      STAMP(3);
      if ((u32)__builtin_amdgcn_readfirstlane(c0) < 4u) lds_wait_ge(FLG_OFF, 4u);
    }
#pragma unroll
    for (int hh = 0; hh < 2; ++hh)
#pragma unroll
      for (int m = 0; m < 4; ++m)
#pragma unroll
        for (int j = 0; j < 4; ++j)
#pragma unroll
          for (int n = 2 * hh; n < 2 * hh + 2; ++n)
            *(LAS float*)(uintptr_t)(((j & 1) ? hw1 : hw0) + (16 * m + 2 * j) * 256 + (n ^ (j & 2)) * 64) = acc[m][n][j];
    asm volatile("ds_write_b32 %0, %1" :: "v"(FLG_OFF + 16 + wq * 4), "v"(one) : "memory");
#pragma unroll
    for (int i = 0; i < 4; ++i) { hv[i][0] = *(const LAS f32x4*)(uintptr_t)(h0 + i * 4096); hv[i][1] = *(const LAS f32x4*)(uintptr_t)(h1 + i * 4096); }
    STAMP(4);
    asm volatile(".Lcons_end:");
  }
  f32x4 av[2], bv[2];
#pragma unroll 1
  for (u32 sp = 0; sp < (1u << 22); ++sp) {
    u32 c;
    asm volatile("ds_read_b32 %0, %5\n\tds_read_b128 %1, %6\n\tds_read_b128 %2, %6 offset:16\n\tds_read_b128 %3, %6 offset:256\n\tds_read_b128 %4, %6 offset:272\n\ts_waitcnt lgkmcnt(0)"
                 : "=&v"(c), "=&v"(av[0]), "=&v"(av[1]), "=&v"(bv[0]), "=&v"(bv[1]) : "v"(abf), "v"(abo) : "memory");
    if ((u32)__builtin_amdgcn_readfirstlane(c) >= 2u) break;
  }
  STAMP(5);
  const __amdgpu_buffer_rsrc_t orr = __builtin_amdgcn_make_buffer_rsrc((void*)outp, 0, (int)(xlen >> 1) - 640, 0x00020000);
  const float K1 = -2.0f * 0.7978845608028654f * 1.4426950408889634f;
  const float K3 = K1 * 0.044715f;
  const f32x2 K1v = {K1, K1}, K3v = {K3, K3}, ONE = {1.f, 1.f};
#pragma unroll
  for (int i = 0; i < 4; ++i) {
    f32x2 z[4], e[4];
#pragma unroll
    for (int j = 0; j < 4; ++j) {
      const f32x2 h2 = (j & 1) ? hv[i][j >> 1].zw : hv[i][j >> 1].xy;
      const f32x2 a2 = (j & 1) ? av[j >> 1].zw : av[j >> 1].xy;
      const f32x2 b2 = (j & 1) ? bv[j >> 1].zw : bv[j >> 1].xy;
      z[j] = __builtin_elementwise_fma(h2, a2, b2);
      e[j] = z[j] * __builtin_elementwise_fma(z[j] * z[j], K3v, K1v);
    }
#pragma unroll
    for (int j = 0; j < 4; ++j) { e[j].x = __builtin_amdgcn_exp2f(e[j].x); e[j].y = __builtin_amdgcn_exp2f(e[j].y); }
#pragma unroll
    for (int j = 0; j < 4; ++j) e[j] += ONE;
#pragma unroll
    for (int j = 0; j < 4; ++j) { e[j].x = __builtin_amdgcn_rcpf(e[j].x); e[j].y = __builtin_amdgcn_rcpf(e[j].y); }
    u32x4 o;
#pragma unroll
    for (int j = 0; j < 4; ++j) {
      const f32x2 y = z[j] * e[j];
      bf16x2 pk = {(__bf16)y.x, (__bf16)y.y};
      o[j] = __builtin_bit_cast(u32, pk);
    }
    __builtin_amdgcn_raw_buffer_store_b128(o, orr, ov + i * 16 * 512, 0, OUT_AUX);
  }
  STAMP(6);
  STAMP(7);
  asm volatile(".Lkernel_end:");
}

extern "C" void kernel_launch(void* const* d_in, const int* in_sizes, int n_in, void* d_out, int out_size,
                              void* d_ws, size_t ws_size, hipStream_t stream) {
  (void)in_sizes; (void)n_in; (void)out_size; (void)ws_size;
  (void)hipFuncSetAttribute((const void*)conv_stem_kernel, hipFuncAttributeMaxDynamicSharedMemorySize, LDS_BYTES);
  hipLaunchKernelGGL(conv_stem_kernel, dim3(256), dim3(512), LDS_BYTES, stream, (const u16*)d_in[0], (const u16*)d_in[1],
                     (const u16*)d_in[2], (const u16*)d_in[3], (u16*)d_out, (char*)d_ws
  );
}
```

```cpp
#include <hip/hip_runtime.h>
#include <hip/hip_bf16.h>
#include <utility>

typedef unsigned short u16;
typedef unsigned int u32;
typedef unsigned long long u64;
typedef __attribute__((ext_vector_type(4))) short s16x4;
typedef __attribute__((ext_vector_type(8))) __bf16 bf16x8;
typedef __attribute__((ext_vector_type(2))) __bf16 bf16x2;
typedef __attribute__((ext_vector_type(4))) float f32x4;
typedef __attribute__((ext_vector_type(2))) float f32x2;
typedef __attribute__((ext_vector_type(4))) u32 u32x4;
typedef __attribute__((ext_vector_type(2))) u32 u32x2;
#define LAS __attribute__((address_space(3)))
#define GAS __attribute__((address_space(1)))

constexpr int T_ = 16000, L_ = 7999;
constexpr int TR = 250;
constexpr int SLOT = 48 * 1024;
constexpr int RED_OFF = 3 * SLOT;
constexpr int AB_OFF = RED_OFF + 2048;
constexpr int FLG_OFF = AB_OFF + 512;
constexpr int PF_OFF = FLG_OFF + 256;
constexpr int LDS_BYTES = PF_OFF + 1280;
constexpr int WS_EPOCH = 0;
constexpr int WS_GRAN = 4096;
constexpr u32 OOB = 0x40000000u;
constexpr int POLL_DELAY0 = 20, POLL_DELAY1 = 16;
constexpr int G_ST = 52, G_W = 7;

#define SB() __builtin_amdgcn_sched_barrier(0)
#define PIN(v) asm volatile("" : "+v"(v))
#define WAIT_LGKM(n) __builtin_amdgcn_s_waitcnt(0xC07F | ((n) << 8))
__device__ __forceinline__ void bar() { asm volatile("" ::: "memory"); __builtin_amdgcn_s_barrier(); asm volatile("" ::: "memory"); }
__device__ __forceinline__ float bf2f(u32 v) { return __uint_as_float(v << 16); }
__device__ __forceinline__ bf16x8 cat(s16x4 a, s16x4 b) { return __builtin_bit_cast(bf16x8, __builtin_shufflevector(a, b, 0, 1, 2, 3, 4, 5, 6, 7)); }
__device__ __forceinline__ void lds_wait_ge(int a, u32 v) {
#pragma unroll 1
  for (u32 sp = 0; sp < (1u << 22); ++sp) {
    u32 c; asm volatile("ds_read_b32 %0, %1\n\ts_waitcnt lgkmcnt(0)" : "=v"(c) : "v"(a) : "memory");
    if ((u32)__builtin_amdgcn_readfirstlane(c) >= v) break;
    __builtin_amdgcn_s_sleep(1);
  }
}

#define TJ(e) ((e) < 4 ? (e) : ((e) == 4 ? 0 : ((e) < 7 ? (e) - 1 : (e) - 6)))
#define TH(e) ((e) == 4 || (e) >= 7 ? 1 : 0)
template <int... I, class F> __device__ __forceinline__ void sfor_(std::integer_sequence<int, I...>, F&& f) { (f(std::integral_constant<int, I>{}), ...); }
template <int N, class F> __device__ __forceinline__ void sfor(F&& f) { sfor_(std::make_integer_sequence<int, N>{}, f); }
#define CI(x) std::integral_constant<int, (x)>{}
struct Frags { bf16x8 a[4]; s16x4 b[4][2]; };

__global__ __launch_bounds__(512, 2) void conv_stem_kernel(const u16* __restrict__ x, const u16* __restrict__ w,
                                                           const u16* __restrict__ gam, const u16* __restrict__ bet,
                                                           u16* __restrict__ out, char* __restrict__ ws) {
  extern __shared__ __attribute__((aligned(16))) char smem_[];
  LAS char* smem = (LAS char*)smem_;
  asm volatile(".Lk_start:");
  const int bid = blockIdx.x;
  const int cg = (bid >> 3) & 3;
  const int rt = (bid & 7) * 8 + (bid >> 5);
  const int b = rt >> 5, t = rt & 31;
  const int l0 = t * TR;
  const int nvalid = (L_ - l0) < TR ? (L_ - l0) : TR;
  const int grp = b * 4 + cg;
  const int wid = __builtin_amdgcn_readfirstlane(threadIdx.x >> 6);
  const int lane = threadIdx.x & 63;
  const int wq = wid & 3;
  int h0, h1, abo, abf, ov;
  f32x4 hv[4][2];
  int xv = wq * 8192 + (lane >> 3) * 1024 + (((lane & 7) ^ (lane >> 4) ^ ((wq & 1) << 2)) << 4);
  int wv = wq * 4096 + (lane >> 3) * 512 + (((lane & 7) ^ (((lane >> 4) & 1) << 1) ^ ((wq & 1) << 2)) << 4);
  u32 xoff = (u32)(b * T_ + 2 * l0) * 512u, xlen = (u32)(2 * nvalid + 1) * 512u;
  const int pfi = (bid >> 5) * 48 + wq * 12 + lane;
  int pfv = lane < 12 ? ((pfi >> 7) * 256 + ((cg & 1) ^ 1) * 128 + (pfi & 127)) * 512 : (int)OOB;
  PIN(xv); PIN(wv); PIN(pfv);
  asm volatile("" : "+s"(xoff), "+s"(xlen));
  SB();
  asm volatile("" :: "s"(x), "s"(w), "s"(gam), "s"(bet), "s"(out), "s"(ws));
  u16* const outp = out + ((size_t)(b * L_ + l0) * 256 + cg * 64);
  auto epi_addr = [&]() {
    const int hf = wid < 4 ? 1 : 0;
    const int rr = lane >> 2, cc = lane & 3, sw = ((rr >> 1) & 1) | (((rr >> 2) & 1) << 3);
    const int hb = (wq >> 1) * 2 * SLOT + (wq & 1) * 16384 + rr * 256;
    h0 = hb + (((8 * hf + 2 * cc) ^ sw) << 4); h1 = h0 ^ 16;
    abo = AB_OFF + hf * 128 + cc * 32;
    abf = FLG_OFF + 8 + hf * 4;
    ov = (wq * 64 + rr) * 512 + hf * 64 + cc * 16;
    PIN(h0); PIN(h1); PIN(abo); PIN(abf); PIN(ov);
  };

  if (__builtin_expect(wid < 4, 1)) {
    const int lw = wq;
    __amdgpu_buffer_rsrc_t xr = __builtin_amdgcn_make_buffer_rsrc((void*)((const char*)x + xoff), 0, xlen, 0x00020000);
    __amdgpu_buffer_rsrc_t wr = __builtin_amdgcn_make_buffer_rsrc((void*)(w + cg * 64), 0, 768 * 512 - cg * 128, 0x00020000);
    const int rot = (cg & 1) * 2;
    LAS char* lbase = smem + lw * 1024;
    auto issue = [&](int u) {
      const int c = ((u >> 1) + rot) & 3, p = u & 1;
      LAS char* slot = lbase + (u % 3) * SLOT;
      if (p == 0) {
#pragma unroll
        for (int i = 0; i < 4; ++i)
          __builtin_amdgcn_raw_ptr_buffer_load_lds(wr, slot + 32768 + i * 4096, 16, wv, (i >> 1) * 262144 + c * 32768 + (i & 1) * 16384, 0, 0);
      } else {
#pragma unroll
        for (int i = 0; i < 2; ++i)
          __builtin_amdgcn_raw_ptr_buffer_load_lds(wr, slot + 32768 + i * 4096, 16, wv, 131072 + c * 32768 + i * 16384, 0, 0);
      }
#pragma unroll
      for (int i = 0; i < 8; ++i)
        __builtin_amdgcn_raw_ptr_buffer_load_lds(xr, slot + i * 4096, 16, xv, i * 32768 + p * 512 + c * 128, 0, 0);
    };
    issue(0);
    const __amdgpu_buffer_rsrc_t gr = __builtin_amdgcn_make_buffer_rsrc((void*)(ws + WS_GRAN + grp * 32768), 0, 32768, 0x00020000);
    const int ch = lw * 16 + (lane & 15);
#pragma unroll
    for (int k = 0; k < 8; ++k) {
      if (k + 1 < 8) {
        issue(k + 1);
        if ((k + 1) & 1) asm volatile("s_waitcnt vmcnt(10)" ::: "memory");
        else asm volatile("s_waitcnt vmcnt(12)" ::: "memory");
      } else {
        asm volatile("s_waitcnt vmcnt(0)" ::: "memory");
      }
      bar();
      if (k == 0) {
        __builtin_amdgcn_raw_ptr_buffer_load_lds(wr, smem + PF_OFF + lw * 256, 4, pfv, 0, 0, 0);
        if (lw == 0 && lane < 2)
          __builtin_amdgcn_global_load_lds((const GAS u32*)(ws + WS_EPOCH + grp * 128 + lane * 4), (LAS u32*)(smem + FLG_OFF + 32), 4, 0, 16);
      }
    }
    u32 g_raw, b_raw, t0_, t1_; u32x2 tg;
    asm volatile("global_load_ushort %0, %5, %6\n\tglobal_load_ushort %1, %5, %7\n\t"
                 "global_load_dword %2, %8, %9\n\tglobal_load_dword %3, %10, %11\n\t"
                 "ds_read_b64 %4, %12\n\ts_waitcnt vmcnt(0) lgkmcnt(0)"
                 : "=&v"(g_raw), "=&v"(b_raw), "=&v"(t0_), "=&v"(t1_), "=&v"(tg)
                 : "v"((cg * 64 + ch) * 2), "s"(gam), "s"(bet), "v"(WS_GRAN - 128), "s"(ws), "v"((lane & 1) * (248 * 512)), "s"(outp), "v"(FLG_OFF + 32) : "memory");
    const u32 tagA = __builtin_amdgcn_readfirstlane(tg.x) + 1u, tagB = __builtin_amdgcn_readfirstlane(tg.y) + 1u;
    epi_addr();
    float g_c = bf2f(g_raw), b_c = bf2f(b_raw);
    int abw = AB_OFF + (((lane >> 4) & 1) * 64 + ch) * 4, abc = FLG_OFF + 8 + (lw >> 1) * 4, one = 1;
    u32 voff = (lane >> 4) * 8192 + ch * 16;
    int hfl = FLG_OFF + 16 + lw * 4;
    PIN(g_c); PIN(b_c); PIN(abw); PIN(voff); PIN(abc); PIN(one); PIN(hfl);
    float s = 0.f, q = 0.f;
    lds_wait_ge(FLG_OFF + (lw >> 1) * 4, 4u);
    if (lw < 2) __builtin_amdgcn_s_sleep(POLL_DELAY0); else __builtin_amdgcn_s_sleep(POLL_DELAY1);
#pragma unroll 1
    for (u32 spin = 0; spin < (1u << 16); ++spin) {
      u32x4 gv[8];
      asm volatile(
          "buffer_load_dwordx4 %0, %8, %9, 0 offen sc1\n\t"
          "buffer_load_dwordx4 %1, %8, %9, 0 offen offset:1024 sc1\n\t"
          "buffer_load_dwordx4 %2, %8, %9, 0 offen offset:2048 sc1\n\t"
          "buffer_load_dwordx4 %3, %8, %9, 0 offen offset:3072 sc1\n\t"
          "buffer_load_dwordx4 %4, %8, %9, %10 offen sc1\n\t"
          "buffer_load_dwordx4 %5, %8, %9, %10 offen offset:1024 sc1\n\t"
          "buffer_load_dwordx4 %6, %8, %9, %10 offen offset:2048 sc1\n\t"
          "buffer_load_dwordx4 %7, %8, %9, %10 offen offset:3072 sc1\n\t"
          "s_waitcnt vmcnt(0)"
          : "=&v"(gv[0]), "=&v"(gv[1]), "=&v"(gv[2]), "=&v"(gv[3]), "=&v"(gv[4]), "=&v"(gv[5]), "=&v"(gv[6]), "=&v"(gv[7])
          : "v"(voff), "s"(gr), "s"(4096)
          : "memory");
      u32 m0 = voff & OOB, m1 = 0;
#pragma unroll
      for (int i = 0; i < 8; ++i) { m0 |= gv[i].y ^ tagA; m1 |= gv[i].w ^ tagB; }
      const float sn = ((__uint_as_float(gv[0].x) + __uint_as_float(gv[1].x)) + (__uint_as_float(gv[2].x) + __uint_as_float(gv[3].x))) +
                       ((__uint_as_float(gv[4].x) + __uint_as_float(gv[5].x)) + (__uint_as_float(gv[6].x) + __uint_as_float(gv[7].x)));
      const float qn = ((__uint_as_float(gv[0].z) + __uint_as_float(gv[1].z)) + (__uint_as_float(gv[2].z) + __uint_as_float(gv[3].z))) +
                       ((__uint_as_float(gv[4].z) + __uint_as_float(gv[5].z)) + (__uint_as_float(gv[6].z) + __uint_as_float(gv[7].z)));
      const bool ok = (m0 | m1) == 0u;
      s = ok ? sn : s; q = ok ? qn : q;
      voff = ok ? OOB : voff;
      if (__builtin_amdgcn_ballot_w64(voff != OOB) == 0ull) break;
      __builtin_amdgcn_s_sleep(1);
    }
    {
      auto r1 = __builtin_amdgcn_permlane32_swap(__float_as_uint(s), __float_as_uint(q), false, false);
      float u = __uint_as_float(r1[0]) + __uint_as_float(r1[1]);
      auto r2 = __builtin_amdgcn_permlane16_swap(__float_as_uint(u), __float_as_uint(u), false, false);
      u = __uint_as_float(r2[0]) + __uint_as_float(r2[1]);
      auto r3 = __builtin_amdgcn_permlane32_swap(__float_as_uint(u), __float_as_uint(u), false, false);
      s = __uint_as_float(r3[0]); q = __uint_as_float(r3[1]);
    }
    const float mu = s * (1.0f / L_);
    const float var = fmaxf(q * (1.0f / L_) - mu * mu, 0.f);
    const float a = g_c * __builtin_amdgcn_rsqf(var + 1e-5f);
    const float abv = (lane & 16) ? b_c - mu * a : a;
    asm volatile("ds_write_b32 %0, %1\n\ts_mov_b64 exec, 1\n\tds_add_u32 %2, %3\n\ts_mov_b64 exec, -1" :: "v"(abw), "v"(abv), "v"(abc), "v"(one) : "memory");
    if (t == 0 && lw == 0 && lane == 0)
      __hip_atomic_store((GAS u64*)(ws + WS_EPOCH + grp * 128), (u64)tagA | ((u64)tagB << 32), __ATOMIC_RELAXED, __HIP_MEMORY_SCOPE_AGENT);
#pragma unroll 1
    for (u32 sp = 0; sp < (1u << 22); ++sp) {
      u32 c;
      asm volatile("ds_read_b32 %0, %9\n\t"
                   "ds_read_b128 %1, %10\n\tds_read_b128 %2, %11\n\tds_read_b128 %3, %10 offset:4096\n\tds_read_b128 %4, %11 offset:4096\n\t"
                   "ds_read_b128 %5, %10 offset:8192\n\tds_read_b128 %6, %11 offset:8192\n\tds_read_b128 %7, %10 offset:12288\n\tds_read_b128 %8, %11 offset:12288\n\t"
                   "s_waitcnt lgkmcnt(0)"
                   : "=&v"(c), "=&v"(hv[0][0]), "=&v"(hv[0][1]), "=&v"(hv[1][0]), "=&v"(hv[1][1]), "=&v"(hv[2][0]), "=&v"(hv[2][1]), "=&v"(hv[3][0]), "=&v"(hv[3][1])
                   : "v"(hfl), "v"(h0), "v"(h1) : "memory");
      if (__builtin_amdgcn_readfirstlane(c) != 0) break;
      __builtin_amdgcn_s_sleep(2);
    }
  } else {
    if (wq == 0) {
      u64 pc; int c0, e1, e2;
      asm volatile("s_getpc_b64 %0\n\t.Lpf_here:\n\ts_mov_b32 %1, .Lk_start-.Lpf_here\n\ts_mov_b32 %2, .Lkernel_end-.Lpf_here\n\ts_mov_b32 %3, .Lcons_end-.Lpf_here"
                   : "=s"(pc), "=s"(c0), "=s"(e1), "=s"(e2));
      const int co = c0 + ((bid >> 3) * 4 + (lane & 3)) * 128;
      __builtin_amdgcn_global_load_lds((const GAS u32*)((const char*)pc + (co < (e1 > e2 ? e1 : e2) ? co : 0)), (LAS u32*)(smem + PF_OFF + 1024), 4, 0, 0);
    }
    char* gp = ws + WS_GRAN + grp * 32768;
    asm volatile("" : "+s"(gp));
    const __amdgpu_buffer_rsrc_t gr = __builtin_amdgcn_make_buffer_rsrc((void*)gp, 0, 32768, 0x00020000);
    if (wq == 0 && lane < 8) *(LAS u32*)(smem + FLG_OFF + lane * 4) = 0u;
    epi_addr();
    const int r = lane & 15, g = lane >> 4;
    const int rho = r < 4 ? 2 * r : (r >= 12 ? 2 * (r - 8) : 2 * (r - 4) + 1);
    int abase[2][2][2], abase3[2][2][2], bbase[2][4];
#pragma unroll
    for (int sh = 0; sh < 2; ++sh) {
      const int i = 64 * wq + rho + sh;
      const int i3 = (64 * wq + 48 + rho < nvalid) ? i + 48 : 254;
#pragma unroll
      for (int s = 0; s < 2; ++s) {
        abase[0][sh][s] = i * 128 + (((4 * s + g) ^ ((i >> 1) & 7)) << 4);
        abase3[0][sh][s] = i3 * 128 + (((4 * s + g) ^ ((i3 >> 1) & 7)) << 4);
        abase[1][sh][s] = abase[0][sh][s] + 2 * SLOT; abase3[1][sh][s] = abase3[0][sh][s] + 2 * SLOT;
        PIN(abase[0][sh][s]); PIN(abase3[0][sh][s]); PIN(abase[1][sh][s]); PIN(abase3[1][sh][s]);
      }
    }
    {
      const int q4 = (lane & 15) >> 2, p4 = lane & 3, sg = (q4 >> 1) + 2 * (g & 1);
#pragma unroll
      for (int n = 0; n < 4; ++n) {
        bbase[0][n] = 32768 + (8 * g + q4) * 128 + ((2 * (n ^ sg) + (p4 >> 1)) << 4) + 8 * (p4 & 1);
        bbase[1][n] = bbase[0][n] + 2 * SLOT;
        PIN(bbase[0][n]); PIN(bbase[1][n]);
      }
    }
    int hw0, hw1, redw = RED_OFF + wq * 512 + ((g >> 1) * 16 + r) * 8 + (g & 1) * 4, redo = RED_OFF + (lane & 31) * 8, flga = FLG_OFF, one = 1;
    u32 gvo = lane < 32 ? lane * 16 + t * 1024 : OOB, gvo1 = gvo + 512;
    int redo1 = redo + 256;
    PIN(gvo1); PIN(redo1);
    {
      const int rg = ((g & 1) ^ (g >> 1)) + 8 * (g >> 1);
      hw0 = (wq >> 1) * 2 * SLOT + (wq & 1) * 16384 + rg * 256 + r * 4;
      hw1 = hw0 ^ 16;
      PIN(hw0); PIN(hw1); PIN(redo); PIN(redw); PIN(gvo); PIN(flga); PIN(one);
    }
    u32 tagA = 0, tagB = 0;
    f32x4 acc[4][4];
#pragma unroll
    for (int m = 0; m < 4; ++m)
#pragma unroll
      for (int n = 0; n < 4; ++n) acc[m][n] = f32x4{0.f, 0.f, 0.f, 0.f};
    auto rdA = [&](int gs, int m) {
      const int pos = gs % 6, u = 2 * (gs / 6) + (pos >= 4 ? 1 : 0), ts = (pos == 2 || pos == 3) ? 1 : 0, s = pos & 1;
      const int hi = (u % 3) == 2, so = (u % 3) == 1 ? SLOT : 0;
      return m < 3 ? *(const LAS bf16x8*)(uintptr_t)(abase[hi][ts][s] + so + m * 2048) : *(const LAS bf16x8*)(uintptr_t)(abase3[hi][ts][s] + so);
    };
    auto rdB = [&](int gs, int n, int tt) {
      const int pos = gs % 6, u = 2 * (gs / 6) + (pos >= 4 ? 1 : 0), ts = (pos == 2 || pos == 3) ? 1 : 0, s = pos & 1;
      const int hi = (u % 3) == 2, so = (u % 3) == 1 ? SLOT : 0;
      return __builtin_amdgcn_ds_read_tr16_b64_v4i16((LAS s16x4*)(uintptr_t)(bbase[hi][n] + so + ts * 8192 + s * 4096 + tt * 512));
    };
    auto rd = [&](Frags& f, int gs, int idx) {
      if (idx == 0) f.a[0] = rdA(gs, 0);
      else if (idx <= 8) f.b[(idx - 1) >> 1][(idx - 1) & 1] = rdB(gs, (idx - 1) >> 1, (idx - 1) & 1);
      else f.a[idx - 8] = rdA(gs, idx - 8);
    };
    Frags tf[6];
    auto rdT = [&](auto kc) __attribute__((always_inline)) {
      constexpr int k = decltype(kc)::value;
      constexpr int e = k < 32 ? k >> 3 : (k < 36 ? 4 : (k < 52 ? 5 + ((k - 36) >> 3) : 7 + ((k - 52) >> 2)));
      constexpr int i = k < 32 ? k & 7 : (k < 36 ? k - 32 : (k < 52 ? (k - 36) & 7 : (k - 52) & 3));
      constexpr int j = TJ(e);
      if constexpr (TH(e)) tf[j].b[2 + (i >> 1)][i & 1] = rdB(18 + j, 2 + (i >> 1), i & 1);
      else if constexpr (i == 0) tf[j].a[0] = rdA(18 + j, 0);
      else if constexpr (i <= 4) tf[j].b[(i - 1) >> 1][(i - 1) & 1] = rdB(18 + j, (i - 1) >> 1, (i - 1) & 1);
      else tf[j].a[i - 4] = rdA(18 + j, i - 4);
    };
    f32x2 s2[4], q2[4];
    auto stat = [&](int m, int n, int part) {
      const f32x2 v = part ? acc[m][n].zw : acc[m][n].xy;
      if (m == 0 && part == 0) { s2[n] = v; q2[n] = v * v; }
      else { s2[n] += v; q2[n] = __builtin_elementwise_fma(v, v, q2[n]); }
    };
    auto red_half = [&](int h) {
      auto xs = __builtin_amdgcn_permlane32_swap(__float_as_uint(s2[2 * h].x + s2[2 * h].y), __float_as_uint(s2[2 * h + 1].x + s2[2 * h + 1].y), false, false);
      auto xq = __builtin_amdgcn_permlane32_swap(__float_as_uint(q2[2 * h].x + q2[2 * h].y), __float_as_uint(q2[2 * h + 1].x + q2[2 * h + 1].y), false, false);
      const float us = __uint_as_float(xs[0]) + __uint_as_float(xs[1]), uq = __uint_as_float(xq[0]) + __uint_as_float(xq[1]);
      auto y = __builtin_amdgcn_permlane16_swap(__float_as_uint(us), __float_as_uint(uq), false, false);
      return __uint_as_float(y[0]) + __uint_as_float(y[1]);
    };
    Frags fr[2];
    __builtin_amdgcn_s_waitcnt(0x0070);
    __builtin_amdgcn_s_setprio(1);
    bar();
#pragma unroll
    for (int i = 0; i < 12; ++i) { rd(fr[0], 0, i); SB(); }
#pragma unroll
    for (int gs = 0; gs < 17; ++gs) {
      Frags& cur = fr[gs & 1];
      Frags& nxt = fr[(gs + 1) & 1];
      const int np = (gs + 1) % 6;
      const bool newunit = np == 0 || np == 4;
      const int lead = newunit ? 4 : 1;
      WAIT_LGKM(3);
      SB();
#pragma unroll
      for (int i = 0; i < 16; ++i) {
        const int m = i >> 2, n = (m & 1) ? 3 - (i & 3) : (i & 3);
        if (i == 4 && !newunit) { WAIT_LGKM(4); SB(); }
        acc[m][n] = __builtin_amdgcn_mfma_f32_16x16x32_bf16(cur.a[m], cat(cur.b[n][0], cur.b[n][1]), acc[m][n], 0, 0, 0);
        SB();
        if (newunit && i == lead - 1) {
          WAIT_LGKM(0);
          bar();
          SB();
        }
        if (i >= lead - 1 && i < lead - 1 + 12) { rd(nxt, gs + 1, i - (lead - 1)); SB(); }
      }
    }
    WAIT_LGKM(3);
    SB();
    sfor<16>([&](auto ic) __attribute__((always_inline)) {
      constexpr int i = decltype(ic)::value, m = i >> 2, n = (m & 1) ? 3 - (i & 3) : (i & 3);
      acc[m][n] = __builtin_amdgcn_mfma_f32_16x16x32_bf16(fr[1].a[m], cat(fr[1].b[n][0], fr[1].b[n][1]), acc[m][n], 0, 0, 0);
      SB();
      if constexpr (i == 3) { WAIT_LGKM(0); bar(); SB(); }
      if constexpr (i >= 3) { rdT(CI(i - 3)); SB(); }
    });
    u32 cnt0; f32x4 ra, rb; u32x2 ep;
    sfor<96>([&](auto gc) __attribute__((always_inline)) {
      constexpr int gidx = decltype(gc)::value, e = gidx >> 3, i = gidx & 7, j = TJ(e), h = TH(e);
      constexpr int m = i >> 1, n = 2 * h + ((i ^ (i >> 1)) & 1);
      acc[m][n] = __builtin_amdgcn_mfma_f32_16x16x32_bf16(tf[j].a[m], cat(tf[j].b[n][0], tf[j].b[n][1]), acc[m][n], 0, 0, 0);
      SB();
      if constexpr (gidx < 23) { rdT(CI(13 + gidx)); SB(); }
      if constexpr (gidx == 23) { ep = *(const LAS u32x2*)(uintptr_t)(flga + 32); SB(); }
      if constexpr (gidx == 44) { tagA = ep.x + 1u; tagB = ep.y + 1u; PIN(tagA); PIN(tagB); SB(); }
      if constexpr (gidx == 24) { bar(); SB(); }
      if constexpr (gidx >= 24 && gidx < 60) { rdT(CI(36 + gidx - 24)); SB(); }
      if constexpr (gidx >= G_ST && gidx < G_ST + 8) { constexpr int k = gidx - G_ST; stat(k >> 1, (k ^ (k >> 1)) & 1, 0); stat(k >> 1, (k ^ (k >> 1)) & 1, 1); SB(); }
      if constexpr (gidx == G_ST + 9) {
        const float pr = red_half(0);
        asm volatile("ds_write_b32 %3, %4\n\t"
                     "s_mov_b64 exec, 1\n\t"
                     "ds_add_rtn_u32 %0, %5, %6\n\t"
                     "s_mov_b64 exec, -1\n\t"
                     "ds_read2st64_b64 %1, %7 offset1:1\n\t"
                     "ds_read2st64_b64 %2, %7 offset0:2 offset1:3"
                     : "=&v"(cnt0), "=&v"(ra), "=&v"(rb) : "v"(redw), "v"(pr), "v"(flga), "v"(one), "v"(redo) : "memory");
        SB();
      }
      if constexpr (gidx == G_ST + 9 + G_W) {
        asm volatile("s_waitcnt lgkmcnt(0)" : "+v"(cnt0), "+v"(ra), "+v"(rb) :: "memory");
        const f32x2 rr = (ra.xy + ra.zw) + (rb.xy + rb.zw);
        u32x4 gv = {__float_as_uint(rr.x), tagA, __float_as_uint(rr.y), tagB};
        __builtin_amdgcn_raw_buffer_store_b128(gv, gr, __builtin_amdgcn_readfirstlane(cnt0) == 3u ? gvo : OOB, 0, 16);
        SB();
      }
      if constexpr (gidx >= 92) { constexpr int k = 2 * (gidx - 92); stat(k >> 2, 2 + (((k >> 1) ^ (k >> 2)) & 1), 0); stat(k >> 2, 2 + (((k >> 1) ^ (k >> 2)) & 1), 1); SB(); }
    });
#pragma unroll
    for (int k = 8; k < 16; ++k) stat(k >> 2, 2 + (((k >> 1) ^ (k >> 2)) & 1), k & 1);
    {
      const float pr = red_half(1);
      u32 old1, c0; f32x4 r01, r23;
      asm volatile("ds_write_b32 %4, %5 offset:256\n\t"
                   "s_mov_b64 exec, 1\n\t"
                   "ds_add_rtn_u32 %0, %6, %7 offset:4\n\t"
                   "s_mov_b64 exec, -1\n\t"
                   "ds_read2st64_b64 %1, %8 offset1:1\n\t"
                   "ds_read2st64_b64 %2, %8 offset0:2 offset1:3\n\t"
                   "ds_read_b32 %3, %6\n\t"
                   "s_waitcnt lgkmcnt(0)"
                   : "=&v"(old1), "=&v"(r01), "=&v"(r23), "=&v"(c0) : "v"(redw), "v"(pr), "v"(flga), "v"(one), "v"(redo1) : "memory");
      const f32x2 rr = (r01.xy + r01.zw) + (r23.xy + r23.zw);
      u32x4 gv = {__float_as_uint(rr.x), tagA, __float_as_uint(rr.y), tagB};
      __builtin_amdgcn_raw_buffer_store_b128(gv, gr, __builtin_amdgcn_readfirstlane(old1) == 3u ? gvo1 : OOB, 0, 16);
      __builtin_amdgcn_s_setprio(0);
      if ((u32)__builtin_amdgcn_readfirstlane(c0) < 4u) lds_wait_ge(FLG_OFF, 4u);
    }
#pragma unroll
    for (int hh = 0; hh < 2; ++hh)
#pragma unroll
      for (int m = 0; m < 4; ++m)
#pragma unroll
        for (int j = 0; j < 4; ++j)
#pragma unroll
          for (int n = 2 * hh; n < 2 * hh + 2; ++n)
            *(LAS float*)(uintptr_t)(((j & 1) ? hw1 : hw0) + (16 * m + 2 * j) * 256 + (n ^ (j & 2)) * 64) = acc[m][n][j];
    asm volatile("ds_write_b32 %0, %1" :: "v"(FLG_OFF + 16 + wq * 4), "v"(one) : "memory");
#pragma unroll
    for (int i = 0; i < 4; ++i) { hv[i][0] = *(const LAS f32x4*)(uintptr_t)(h0 + i * 4096); hv[i][1] = *(const LAS f32x4*)(uintptr_t)(h1 + i * 4096); }
    asm volatile(".Lcons_end:");
  }
  f32x4 av[2], bv[2];
#pragma unroll 1
  for (u32 sp = 0; sp < (1u << 22); ++sp) {
    u32 c;
    asm volatile("ds_read_b32 %0, %5\n\tds_read_b128 %1, %6\n\tds_read_b128 %2, %6 offset:16\n\tds_read_b128 %3, %6 offset:256\n\tds_read_b128 %4, %6 offset:272\n\ts_waitcnt lgkmcnt(0)"
                 : "=&v"(c), "=&v"(av[0]), "=&v"(av[1]), "=&v"(bv[0]), "=&v"(bv[1]) : "v"(abf), "v"(abo) : "memory");
    if ((u32)__builtin_amdgcn_readfirstlane(c) >= 2u) break;
  }
  const __amdgpu_buffer_rsrc_t orr = __builtin_amdgcn_make_buffer_rsrc((void*)outp, 0, (int)(xlen >> 1) - 640, 0x00020000);
  const float K1 = -2.0f * 0.7978845608028654f * 1.4426950408889634f;
  const float K3 = K1 * 0.044715f;
  const f32x2 K1v = {K1, K1}, K3v = {K3, K3}, ONE = {1.f, 1.f};
#pragma unroll
  for (int i = 0; i < 4; ++i) {
    f32x2 z[4], e[4];
#pragma unroll
    for (int j = 0; j < 4; ++j) {
      const f32x2 h2 = (j & 1) ? hv[i][j >> 1].zw : hv[i][j >> 1].xy;
      const f32x2 a2 = (j & 1) ? av[j >> 1].zw : av[j >> 1].xy;
      const f32x2 b2 = (j & 1) ? bv[j >> 1].zw : bv[j >> 1].xy;
      z[j] = __builtin_elementwise_fma(h2, a2, b2);
      e[j] = z[j] * __builtin_elementwise_fma(z[j] * z[j], K3v, K1v);
    }
#pragma unroll
    for (int j = 0; j < 4; ++j) { e[j].x = __builtin_amdgcn_exp2f(e[j].x); e[j].y = __builtin_amdgcn_exp2f(e[j].y); }
#pragma unroll
    for (int j = 0; j < 4; ++j) e[j] += ONE;
#pragma unroll
    for (int j = 0; j < 4; ++j) { e[j].x = __builtin_amdgcn_rcpf(e[j].x); e[j].y = __builtin_amdgcn_rcpf(e[j].y); }
    u32x4 o;
#pragma unroll
    for (int j = 0; j < 4; ++j) {
      const f32x2 y = z[j] * e[j];
      bf16x2 pk = {(__bf16)y.x, (__bf16)y.y};
      o[j] = __builtin_bit_cast(u32, pk);
    }
    __builtin_amdgcn_raw_buffer_store_b128(o, orr, ov + i * 16 * 512, 0, 16);
  }
  asm volatile(".Lkernel_end:");
}

extern "C" void kernel_launch(void* const* d_in, const int* in_sizes, int n_in, void* d_out, int out_size,
                              void* d_ws, size_t ws_size, hipStream_t stream) {
  (void)in_sizes; (void)n_in; (void)out_size; (void)ws_size;
  (void)hipFuncSetAttribute((const void*)conv_stem_kernel, hipFuncAttributeMaxDynamicSharedMemorySize, LDS_BYTES);
  hipLaunchKernelGGL(conv_stem_kernel, dim3(256), dim3(512), LDS_BYTES, stream, (const u16*)d_in[0], (const u16*)d_in[1],
                     (const u16*)d_in[2], (const u16*)d_in[3], (u16*)d_out, (char*)d_ws);
}
```

```cpp
#include <hip/hip_runtime.h>
#include <hip/hip_bf16.h>
#include <utility>

typedef unsigned short u16;
typedef unsigned int u32;
typedef unsigned long long u64;
typedef __attribute__((ext_vector_type(4))) short s16x4;
typedef __attribute__((ext_vector_type(8))) __bf16 bf16x8;
typedef __attribute__((ext_vector_type(2))) __bf16 bf16x2;
typedef __attribute__((ext_vector_type(4))) float f32x4;
typedef __attribute__((ext_vector_type(2))) float f32x2;
typedef __attribute__((ext_vector_type(4))) u32 u32x4;
typedef __attribute__((ext_vector_type(2))) u32 u32x2;
#define LAS __attribute__((address_space(3)))
#define GAS __attribute__((address_space(1)))

constexpr int T_ = 16000, L_ = 7999;
constexpr int TR = 250;
constexpr int SLOT = 48 * 1024;
constexpr int RED_OFF = 3 * SLOT;
constexpr int AB_OFF = RED_OFF + 2048;
constexpr int FLG_OFF = AB_OFF + 512;
constexpr int PF_OFF = FLG_OFF + 256;
constexpr int LDS_BYTES = PF_OFF + 1280;
constexpr int WS_EPOCH = 0;
constexpr int WS_GRAN = 4096;
constexpr u32 OOB = 0x40000000u;
constexpr int POLL_DELAY0 = 20, POLL_DELAY1 = 16;
constexpr int G_ST = 52, G_W = 7;

#define SB() __builtin_amdgcn_sched_barrier(0)
#define PIN(v) asm volatile("" : "+v"(v))
#define WAIT_LGKM(n) __builtin_amdgcn_s_waitcnt(0xC07F | ((n) << 8))
__device__ __forceinline__ void bar() { asm volatile("" ::: "memory"); __builtin_amdgcn_s_barrier(); asm volatile("" ::: "memory"); }
__device__ __forceinline__ float bf2f(u32 v) { return __uint_as_float(v << 16); }
__device__ __forceinline__ bf16x8 cat(s16x4 a, s16x4 b) { return __builtin_bit_cast(bf16x8, __builtin_shufflevector(a, b, 0, 1, 2, 3, 4, 5, 6, 7)); }
__device__ __forceinline__ void lds_wait_ge(int a, u32 v) {
#pragma unroll 1
  for (u32 sp = 0; sp < (1u << 22); ++sp) {
    u32 c; asm volatile("ds_read_b32 %0, %1\n\ts_waitcnt lgkmcnt(0)" : "=v"(c) : "v"(a) : "memory");
    if ((u32)__builtin_amdgcn_readfirstlane(c) >= v) break;
    __builtin_amdgcn_s_sleep(1);
  }
}

#define TJ(e) ((e) < 4 ? (e) : ((e) == 4 ? 0 : ((e) < 7 ? (e) - 1 : (e) - 6)))
#define TH(e) ((e) == 4 || (e) >= 7 ? 1 : 0)
template <int... I, class F> __device__ __forceinline__ void sfor_(std::integer_sequence<int, I...>, F&& f) { (f(std::integral_constant<int, I>{}), ...); }
template <int N, class F> __device__ __forceinline__ void sfor(F&& f) { sfor_(std::make_integer_sequence<int, N>{}, f); }
#define CI(x) std::integral_constant<int, (x)>{}
struct Frags { bf16x8 a[4]; s16x4 b[4][2]; };

__global__ __launch_bounds__(512, 2) void conv_stem_kernel(const u16* __restrict__ x, const u16* __restrict__ w,
                                                           const u16* __restrict__ gam, const u16* __restrict__ bet,
                                                           u16* __restrict__ out, char* __restrict__ ws) {
  extern __shared__ __attribute__((aligned(16))) char smem_[];
  LAS char* smem = (LAS char*)smem_;
  asm volatile(".Lk_start:");
  const int bid = blockIdx.x;
  const int cg = (bid >> 3) & 3;
  const int rt = (bid & 7) * 8 + (bid >> 5);
  const int b = rt >> 5, t = rt & 31;
  const int l0 = t * TR;
  const int nvalid = (L_ - l0) < TR ? (L_ - l0) : TR;
  const int grp = b * 4 + cg;
  const int wid = __builtin_amdgcn_readfirstlane(threadIdx.x >> 6);
  const int lane = threadIdx.x & 63;
  const int wq = wid & 3;
  int h0, h1, abo, abf, ov;
  f32x4 hv[4][2];
  int xv = wq * 8192 + (lane >> 3) * 1024 + (((lane & 7) ^ (lane >> 4) ^ ((wq & 1) << 2)) << 4);
  int wv = wq * 4096 + (lane >> 3) * 512 + (((lane & 7) ^ (((lane >> 4) & 1) << 1) ^ ((wq & 1) << 2)) << 4);
  u32 xoff = (u32)(b * T_ + 2 * l0) * 512u, xlen = (u32)(2 * nvalid + 1) * 512u;
  const int pfi = (bid >> 5) * 48 + wq * 12 + lane;
  int pfv = lane < 12 ? ((pfi >> 7) * 256 + ((cg & 1) ^ 1) * 128 + (pfi & 127)) * 512 : (int)OOB;
  PIN(xv); PIN(wv); PIN(pfv);
  asm volatile("" : "+s"(xoff), "+s"(xlen));
  SB();
  asm volatile("" :: "s"(x), "s"(w), "s"(gam), "s"(bet), "s"(out), "s"(ws));
  u16* const outp = out + ((size_t)(b * L_ + l0) * 256 + cg * 64);
  auto epi_addr = [&]() {
    const int hf = wid < 4 ? 1 : 0;
    const int rr = lane >> 2, cc = lane & 3, sw = ((rr >> 1) & 1) | (((rr >> 2) & 1) << 3);
    const int hb = (wq >> 1) * 2 * SLOT + (wq & 1) * 16384 + rr * 256;
    h0 = hb + (((8 * hf + 2 * cc) ^ sw) << 4); h1 = h0 ^ 16;
    abo = AB_OFF + hf * 128 + cc * 32;
    abf = FLG_OFF + 8 + hf * 4;
    ov = (wq * 64 + rr) * 512 + hf * 64 + cc * 16;
    PIN(h0); PIN(h1); PIN(abo); PIN(abf); PIN(ov);
  };

  if (__builtin_expect(wid < 4, 1)) {
    const int lw = wq;
    __amdgpu_buffer_rsrc_t xr = __builtin_amdgcn_make_buffer_rsrc((void*)((const char*)x + xoff), 0, xlen, 0x00020000);
    __amdgpu_buffer_rsrc_t wr = __builtin_amdgcn_make_buffer_rsrc((void*)(w + cg * 64), 0, 768 * 512 - cg * 128, 0x00020000);
    const int rot = (cg & 1) * 2;
    LAS char* lbase = smem + lw * 1024;
    auto issue = [&](int u) {
      const int c = ((u >> 1) + rot) & 3, p = u & 1;
      LAS char* slot = lbase + (u % 3) * SLOT;
      if (p == 0) {
#pragma unroll
        for (int i = 0; i < 4; ++i)
          __builtin_amdgcn_raw_ptr_buffer_load_lds(wr, slot + 32768 + i * 4096, 16, wv, (i >> 1) * 262144 + c * 32768 + (i & 1) * 16384, 0, 0);
      } else {
#pragma unroll
        for (int i = 0; i < 2; ++i)
          __builtin_amdgcn_raw_ptr_buffer_load_lds(wr, slot + 32768 + i * 4096, 16, wv, 131072 + c * 32768 + i * 16384, 0, 0);
      }
#pragma unroll
      for (int i = 0; i < 8; ++i)
        __builtin_amdgcn_raw_ptr_buffer_load_lds(xr, slot + i * 4096, 16, xv, i * 32768 + p * 512 + c * 128, 0, 0);
    };
    issue(0);
    const __amdgpu_buffer_rsrc_t gr = __builtin_amdgcn_make_buffer_rsrc((void*)(ws + WS_GRAN + grp * 32768), 0, 32768, 0x00020000);
    const int ch = lw * 16 + (lane & 15);
#pragma unroll
    for (int k = 0; k < 8; ++k) {
      if (k + 1 < 8) {
        issue(k + 1);
        if ((k + 1) & 1) asm volatile("s_waitcnt vmcnt(10)" ::: "memory");
        else asm volatile("s_waitcnt vmcnt(12)" ::: "memory");
      } else {
        asm volatile("s_waitcnt vmcnt(0)" ::: "memory");
      }
      bar();
      if (k == 0) {
        __builtin_amdgcn_raw_ptr_buffer_load_lds(wr, smem + PF_OFF + lw * 256, 4, pfv, 0, 0, 0);
        if (lw == 1) {
          u64 pc; int c0, e1, e2;
          asm volatile("s_getpc_b64 %0\n\t.Lpf_here:\n\ts_mov_b32 %1, .Lk_start-.Lpf_here\n\ts_mov_b32 %2, .Lkernel_end-.Lpf_here\n\ts_mov_b32 %3, .Lcons_end-.Lpf_here"
                       : "=s"(pc), "=s"(c0), "=s"(e1), "=s"(e2));
          const int co = c0 + ((bid >> 3) * 4 + (lane & 3)) * 128;
          __builtin_amdgcn_global_load_lds((const GAS u32*)((const char*)pc + (co < (e1 > e2 ? e1 : e2) ? co : 0)), (LAS u32*)(smem + PF_OFF + 1024), 4, 0, 0);
        }
        if (lw == 0 && lane < 2)
          __builtin_amdgcn_global_load_lds((const GAS u32*)(ws + WS_EPOCH + grp * 128 + lane * 4), (LAS u32*)(smem + FLG_OFF + 32), 4, 0, 16);
      }
    }
    u32 g_raw, b_raw, t0_, t1_; u32x2 tg;
    asm volatile("global_load_ushort %0, %5, %6\n\tglobal_load_ushort %1, %5, %7\n\t"
                 "global_load_dword %2, %8, %9\n\tglobal_load_dword %3, %10, %11\n\t"
                 "ds_read_b64 %4, %12\n\ts_waitcnt vmcnt(0) lgkmcnt(0)"
                 : "=&v"(g_raw), "=&v"(b_raw), "=&v"(t0_), "=&v"(t1_), "=&v"(tg)
                 : "v"((cg * 64 + ch) * 2), "s"(gam), "s"(bet), "v"(WS_GRAN - 128), "s"(ws), "v"((lane & 1) * (248 * 512)), "s"(outp), "v"(FLG_OFF + 32) : "memory");
    const u32 tagA = __builtin_amdgcn_readfirstlane(tg.x) + 1u, tagB = __builtin_amdgcn_readfirstlane(tg.y) + 1u;
    epi_addr();
    float g_c = bf2f(g_raw), b_c = bf2f(b_raw);
    int abw = AB_OFF + (((lane >> 4) & 1) * 64 + ch) * 4, abc = FLG_OFF + 8 + (lw >> 1) * 4, one = 1;
    u32 voff = (lane >> 4) * 8192 + ch * 16;
    int hfl = FLG_OFF + 16 + lw * 4;
    PIN(g_c); PIN(b_c); PIN(abw); PIN(voff); PIN(abc); PIN(one); PIN(hfl);
    float s = 0.f, q = 0.f;
    lds_wait_ge(FLG_OFF + (lw >> 1) * 4, 4u);
    if (lw < 2) __builtin_amdgcn_s_sleep(POLL_DELAY0); else __builtin_amdgcn_s_sleep(POLL_DELAY1);
#pragma unroll 1
    for (u32 spin = 0; spin < (1u << 16); ++spin) {
      u32x4 gv[8];
      asm volatile(
          "buffer_load_dwordx4 %0, %8, %9, 0 offen sc1\n\t"
          "buffer_load_dwordx4 %1, %8, %9, 0 offen offset:1024 sc1\n\t"
          "buffer_load_dwordx4 %2, %8, %9, 0 offen offset:2048 sc1\n\t"
          "buffer_load_dwordx4 %3, %8, %9, 0 offen offset:3072 sc1\n\t"
          "buffer_load_dwordx4 %4, %8, %9, %10 offen sc1\n\t"
          "buffer_load_dwordx4 %5, %8, %9, %10 offen offset:1024 sc1\n\t"
          "buffer_load_dwordx4 %6, %8, %9, %10 offen offset:2048 sc1\n\t"
          "buffer_load_dwordx4 %7, %8, %9, %10 offen offset:3072 sc1\n\t"
          "s_waitcnt vmcnt(0)"
          : "=&v"(gv[0]), "=&v"(gv[1]), "=&v"(gv[2]), "=&v"(gv[3]), "=&v"(gv[4]), "=&v"(gv[5]), "=&v"(gv[6]), "=&v"(gv[7])
          : "v"(voff), "s"(gr), "s"(4096)
          : "memory");
      u32 m0 = voff & OOB, m1 = 0;
#pragma unroll
      for (int i = 0; i < 8; ++i) { m0 |= gv[i].y ^ tagA; m1 |= gv[i].w ^ tagB; }
      const float sn = ((__uint_as_float(gv[0].x) + __uint_as_float(gv[1].x)) + (__uint_as_float(gv[2].x) + __uint_as_float(gv[3].x))) +
                       ((__uint_as_float(gv[4].x) + __uint_as_float(gv[5].x)) + (__uint_as_float(gv[6].x) + __uint_as_float(gv[7].x)));
      const float qn = ((__uint_as_float(gv[0].z) + __uint_as_float(gv[1].z)) + (__uint_as_float(gv[2].z) + __uint_as_float(gv[3].z))) +
                       ((__uint_as_float(gv[4].z) + __uint_as_float(gv[5].z)) + (__uint_as_float(gv[6].z) + __uint_as_float(gv[7].z)));
      const bool ok = (m0 | m1) == 0u;
      s = ok ? sn : s; q = ok ? qn : q;
      voff = ok ? OOB : voff;
      if (__builtin_amdgcn_ballot_w64(voff != OOB) == 0ull) break;
      __builtin_amdgcn_s_sleep(1);
    }
    {
      auto r1 = __builtin_amdgcn_permlane32_swap(__float_as_uint(s), __float_as_uint(q), false, false);
      float u = __uint_as_float(r1[0]) + __uint_as_float(r1[1]);
      auto r2 = __builtin_amdgcn_permlane16_swap(__float_as_uint(u), __float_as_uint(u), false, false);
      u = __uint_as_float(r2[0]) + __uint_as_float(r2[1]);
      auto r3 = __builtin_amdgcn_permlane32_swap(__float_as_uint(u), __float_as_uint(u), false, false);
      s = __uint_as_float(r3[0]); q = __uint_as_float(r3[1]);
    }
    const float mu = s * (1.0f / L_);
    const float var = fmaxf(q * (1.0f / L_) - mu * mu, 0.f);
    const float a = g_c * __builtin_amdgcn_rsqf(var + 1e-5f);
    const float abv = (lane & 16) ? b_c - mu * a : a;
    asm volatile("ds_write_b32 %0, %1\n\ts_mov_b64 exec, 1\n\tds_add_u32 %2, %3\n\ts_mov_b64 exec, -1" :: "v"(abw), "v"(abv), "v"(abc), "v"(one) : "memory");
    if (t == 0 && lw == 0 && lane == 0)
      __hip_atomic_store((GAS u64*)(ws + WS_EPOCH + grp * 128), (u64)tagA | ((u64)tagB << 32), __ATOMIC_RELAXED, __HIP_MEMORY_SCOPE_AGENT);
#pragma unroll 1
    for (u32 sp = 0; sp < (1u << 22); ++sp) {
      u32 c;
      asm volatile("ds_read_b32 %0, %9\n\t"
                   "ds_read_b128 %1, %10\n\tds_read_b128 %2, %11\n\tds_read_b128 %3, %10 offset:4096\n\tds_read_b128 %4, %11 offset:4096\n\t"
                   "ds_read_b128 %5, %10 offset:8192\n\tds_read_b128 %6, %11 offset:8192\n\tds_read_b128 %7, %10 offset:12288\n\tds_read_b128 %8, %11 offset:12288\n\t"
                   "s_waitcnt lgkmcnt(0)"
                   : "=&v"(c), "=&v"(hv[0][0]), "=&v"(hv[0][1]), "=&v"(hv[1][0]), "=&v"(hv[1][1]), "=&v"(hv[2][0]), "=&v"(hv[2][1]), "=&v"(hv[3][0]), "=&v"(hv[3][1])
                   : "v"(hfl), "v"(h0), "v"(h1) : "memory");
      if (__builtin_amdgcn_readfirstlane(c) != 0) break;
      __builtin_amdgcn_s_sleep(2);
    }
  } else {
    char* gp = ws + WS_GRAN + grp * 32768;
    asm volatile("" : "+s"(gp));
    const __amdgpu_buffer_rsrc_t gr = __builtin_amdgcn_make_buffer_rsrc((void*)gp, 0, 32768, 0x00020000);
    if (wq == 0 && lane < 8) *(LAS u32*)(smem + FLG_OFF + lane * 4) = 0u;
    epi_addr();
    const int r = lane & 15, g = lane >> 4;
    const int rho = r < 4 ? 2 * r : (r >= 12 ? 2 * (r - 8) : 2 * (r - 4) + 1);
    int abase[2][2][2], abase3[2][2][2], bbase[2][4];
#pragma unroll
    for (int sh = 0; sh < 2; ++sh) {
      const int i = 64 * wq + rho + sh;
      const int i3 = (64 * wq + 48 + rho < nvalid) ? i + 48 : 254;
#pragma unroll
      for (int s = 0; s < 2; ++s) {
        abase[0][sh][s] = i * 128 + (((4 * s + g) ^ ((i >> 1) & 7)) << 4);
        abase3[0][sh][s] = i3 * 128 + (((4 * s + g) ^ ((i3 >> 1) & 7)) << 4);
        abase[1][sh][s] = abase[0][sh][s] + 2 * SLOT; abase3[1][sh][s] = abase3[0][sh][s] + 2 * SLOT;
        PIN(abase[0][sh][s]); PIN(abase3[0][sh][s]); PIN(abase[1][sh][s]); PIN(abase3[1][sh][s]);
      }
    }
    {
      const int q4 = (lane & 15) >> 2, p4 = lane & 3, sg = (q4 >> 1) + 2 * (g & 1);
#pragma unroll
      for (int n = 0; n < 4; ++n) {
        bbase[0][n] = 32768 + (8 * g + q4) * 128 + ((2 * (n ^ sg) + (p4 >> 1)) << 4) + 8 * (p4 & 1);
        bbase[1][n] = bbase[0][n] + 2 * SLOT;
        PIN(bbase[0][n]); PIN(bbase[1][n]);
      }
    }
    int hw0, hw1, redw = RED_OFF + wq * 512 + ((g >> 1) * 16 + r) * 8 + (g & 1) * 4, redo = RED_OFF + (lane & 31) * 8, flga = FLG_OFF, one = 1;
    u32 gvo = lane < 32 ? lane * 16 + t * 1024 : OOB, gvo1 = gvo + 512;
    int redo1 = redo + 256;
    PIN(gvo1); PIN(redo1);
    {
      const int rg = ((g & 1) ^ (g >> 1)) + 8 * (g >> 1);
      hw0 = (wq >> 1) * 2 * SLOT + (wq & 1) * 16384 + rg * 256 + r * 4;
      hw1 = hw0 ^ 16;
      PIN(hw0); PIN(hw1); PIN(redo); PIN(redw); PIN(gvo); PIN(flga); PIN(one);
    }
    u32 tagA = 0, tagB = 0;
    f32x4 acc[4][4];
#pragma unroll
    for (int m = 0; m < 4; ++m)
#pragma unroll
      for (int n = 0; n < 4; ++n) acc[m][n] = f32x4{0.f, 0.f, 0.f, 0.f};
    auto rdA = [&](int gs, int m) {
      const int pos = gs % 6, u = 2 * (gs / 6) + (pos >= 4 ? 1 : 0), ts = (pos == 2 || pos == 3) ? 1 : 0, s = pos & 1;
      const int hi = (u % 3) == 2, so = (u % 3) == 1 ? SLOT : 0;
      return m < 3 ? *(const LAS bf16x8*)(uintptr_t)(abase[hi][ts][s] + so + m * 2048) : *(const LAS bf16x8*)(uintptr_t)(abase3[hi][ts][s] + so);
    };
    auto rdB = [&](int gs, int n, int tt) {
      const int pos = gs % 6, u = 2 * (gs / 6) + (pos >= 4 ? 1 : 0), ts = (pos == 2 || pos == 3) ? 1 : 0, s = pos & 1;
      const int hi = (u % 3) == 2, so = (u % 3) == 1 ? SLOT : 0;
      return __builtin_amdgcn_ds_read_tr16_b64_v4i16((LAS s16x4*)(uintptr_t)(bbase[hi][n] + so + ts * 8192 + s * 4096 + tt * 512));
    };
    auto rd = [&](Frags& f, int gs, int idx) {
      if (idx == 0) f.a[0] = rdA(gs, 0);
      else if (idx <= 8) f.b[(idx - 1) >> 1][(idx - 1) & 1] = rdB(gs, (idx - 1) >> 1, (idx - 1) & 1);
      else f.a[idx - 8] = rdA(gs, idx - 8);
    };
    Frags tf[6];
    auto rdT = [&](auto kc) __attribute__((always_inline)) {
      constexpr int k = decltype(kc)::value;
      constexpr int e = k < 32 ? k >> 3 : (k < 36 ? 4 : (k < 52 ? 5 + ((k - 36) >> 3) : 7 + ((k - 52) >> 2)));
      constexpr int i = k < 32 ? k & 7 : (k < 36 ? k - 32 : (k < 52 ? (k - 36) & 7 : (k - 52) & 3));
      constexpr int j = TJ(e);
      if constexpr (TH(e)) tf[j].b[2 + (i >> 1)][i & 1] = rdB(18 + j, 2 + (i >> 1), i & 1);
      else if constexpr (i == 0) tf[j].a[0] = rdA(18 + j, 0);
      else if constexpr (i <= 4) tf[j].b[(i - 1) >> 1][(i - 1) & 1] = rdB(18 + j, (i - 1) >> 1, (i - 1) & 1);
      else tf[j].a[i - 4] = rdA(18 + j, i - 4);
    };
    f32x2 s2[4], q2[4];
    auto stat = [&](int m, int n, int part) {
      const f32x2 v = part ? acc[m][n].zw : acc[m][n].xy;
      if (m == 0 && part == 0) { s2[n] = v; q2[n] = v * v; }
      else { s2[n] += v; q2[n] = __builtin_elementwise_fma(v, v, q2[n]); }
    };
    auto red_half = [&](int h) {
      auto xs = __builtin_amdgcn_permlane32_swap(__float_as_uint(s2[2 * h].x + s2[2 * h].y), __float_as_uint(s2[2 * h + 1].x + s2[2 * h + 1].y), false, false);
      auto xq = __builtin_amdgcn_permlane32_swap(__float_as_uint(q2[2 * h].x + q2[2 * h].y), __float_as_uint(q2[2 * h + 1].x + q2[2 * h + 1].y), false, false);
      const float us = __uint_as_float(xs[0]) + __uint_as_float(xs[1]), uq = __uint_as_float(xq[0]) + __uint_as_float(xq[1]);
      auto y = __builtin_amdgcn_permlane16_swap(__float_as_uint(us), __float_as_uint(uq), false, false);
      return __uint_as_float(y[0]) + __uint_as_float(y[1]);
    };
    Frags fr[2];
    __builtin_amdgcn_s_waitcnt(0x0070);
    __builtin_amdgcn_s_setprio(1);
    bar();
#pragma unroll
    for (int i = 0; i < 12; ++i) { rd(fr[0], 0, i); SB(); }
#pragma unroll
    for (int gs = 0; gs < 17; ++gs) {
      Frags& cur = fr[gs & 1];
      Frags& nxt = fr[(gs + 1) & 1];
      const int np = (gs + 1) % 6;
      const bool newunit = np == 0 || np == 4;
      const int lead = newunit ? 4 : 1;
      WAIT_LGKM(3);
      SB();
#pragma unroll
      for (int i = 0; i < 16; ++i) {
        const int m = i >> 2, n = (m & 1) ? 3 - (i & 3) : (i & 3);
        if (i == 4 && !newunit) { WAIT_LGKM(4); SB(); }
        acc[m][n] = __builtin_amdgcn_mfma_f32_16x16x32_bf16(cur.a[m], cat(cur.b[n][0], cur.b[n][1]), acc[m][n], 0, 0, 0);
        SB();
        if (newunit && i == lead - 1) {
          WAIT_LGKM(0);
          bar();
          SB();
        }
        if (i >= lead - 1 && i < lead - 1 + 12) { rd(nxt, gs + 1, i - (lead - 1)); SB(); }
      }
    }
    WAIT_LGKM(3);
    SB();
    sfor<16>([&](auto ic) __attribute__((always_inline)) {
      constexpr int i = decltype(ic)::value, m = i >> 2, n = (m & 1) ? 3 - (i & 3) : (i & 3);
      acc[m][n] = __builtin_amdgcn_mfma_f32_16x16x32_bf16(fr[1].a[m], cat(fr[1].b[n][0], fr[1].b[n][1]), acc[m][n], 0, 0, 0);
      SB();
      if constexpr (i == 3) { WAIT_LGKM(0); bar(); SB(); }
      if constexpr (i >= 3) { rdT(CI(i - 3)); SB(); }
    });
    u32 cnt0; f32x4 ra, rb; u32x2 ep;
    sfor<96>([&](auto gc) __attribute__((always_inline)) {
      constexpr int gidx = decltype(gc)::value, e = gidx >> 3, i = gidx & 7, j = TJ(e), h = TH(e);
      constexpr int m = i >> 1, n = 2 * h + ((i ^ (i >> 1)) & 1);
      acc[m][n] = __builtin_amdgcn_mfma_f32_16x16x32_bf16(tf[j].a[m], cat(tf[j].b[n][0], tf[j].b[n][1]), acc[m][n], 0, 0, 0);
      SB();
      if constexpr (gidx < 23) { rdT(CI(13 + gidx)); SB(); }
      if constexpr (gidx == 23) { ep = *(const LAS u32x2*)(uintptr_t)(flga + 32); SB(); }
      if constexpr (gidx == 44) { tagA = ep.x + 1u; tagB = ep.y + 1u; PIN(tagA); PIN(tagB); SB(); }
      if constexpr (gidx == 24) { bar(); SB(); }
      if constexpr (gidx >= 24 && gidx < 60) { rdT(CI(36 + gidx - 24)); SB(); }
      if constexpr (gidx >= G_ST && gidx < G_ST + 8) { constexpr int k = gidx - G_ST; stat(k >> 1, (k ^ (k >> 1)) & 1, 0); stat(k >> 1, (k ^ (k >> 1)) & 1, 1); SB(); }
      if constexpr (gidx == G_ST + 9) {
        const float pr = red_half(0);
        asm volatile("ds_write_b32 %3, %4\n\t"
                     "s_mov_b64 exec, 1\n\t"
                     "ds_add_rtn_u32 %0, %5, %6\n\t"
                     "s_mov_b64 exec, -1\n\t"
                     "ds_read2st64_b64 %1, %7 offset1:1\n\t"
                     "ds_read2st64_b64 %2, %7 offset0:2 offset1:3"
                     : "=&v"(cnt0), "=&v"(ra), "=&v"(rb) : "v"(redw), "v"(pr), "v"(flga), "v"(one), "v"(redo) : "memory");
        SB();
      }
      if constexpr (gidx == G_ST + 9 + G_W) {
        asm volatile("s_waitcnt lgkmcnt(0)" : "+v"(cnt0), "+v"(ra), "+v"(rb) :: "memory");
        const f32x2 rr = (ra.xy + ra.zw) + (rb.xy + rb.zw);
        u32x4 gv = {__float_as_uint(rr.x), tagA, __float_as_uint(rr.y), tagB};
        __builtin_amdgcn_raw_buffer_store_b128(gv, gr, __builtin_amdgcn_readfirstlane(cnt0) == 3u ? gvo : OOB, 0, 16);
        SB();
      }
      if constexpr (gidx >= 92) { constexpr int k = 2 * (gidx - 92); stat(k >> 2, 2 + (((k >> 1) ^ (k >> 2)) & 1), 0); stat(k >> 2, 2 + (((k >> 1) ^ (k >> 2)) & 1), 1); SB(); }
    });
#pragma unroll
    for (int k = 8; k < 16; ++k) stat(k >> 2, 2 + (((k >> 1) ^ (k >> 2)) & 1), k & 1);
    {
      const float pr = red_half(1);
      u32 old1, c0; f32x4 r01, r23;
      asm volatile("ds_write_b32 %4, %5 offset:256\n\t"
                   "s_mov_b64 exec, 1\n\t"
                   "ds_add_rtn_u32 %0, %6, %7 offset:4\n\t"
                   "s_mov_b64 exec, -1\n\t"
                   "ds_read2st64_b64 %1, %8 offset1:1\n\t"
                   "ds_read2st64_b64 %2, %8 offset0:2 offset1:3\n\t"
                   "ds_read_b32 %3, %6\n\t"
                   "s_waitcnt lgkmcnt(0)"
                   : "=&v"(old1), "=&v"(r01), "=&v"(r23), "=&v"(c0) : "v"(redw), "v"(pr), "v"(flga), "v"(one), "v"(redo1) : "memory");
      const f32x2 rr = (r01.xy + r01.zw) + (r23.xy + r23.zw);
      u32x4 gv = {__float_as_uint(rr.x), tagA, __float_as_uint(rr.y), tagB};
      __builtin_amdgcn_raw_buffer_store_b128(gv, gr, __builtin_amdgcn_readfirstlane(old1) == 3u ? gvo1 : OOB, 0, 16);
      __builtin_amdgcn_s_setprio(0);
      if ((u32)__builtin_amdgcn_readfirstlane(c0) < 4u) lds_wait_ge(FLG_OFF, 4u);
    }
#pragma unroll
    for (int hh = 0; hh < 2; ++hh)
#pragma unroll
      for (int m = 0; m < 4; ++m)
#pragma unroll
        for (int j = 0; j < 4; ++j)
#pragma unroll
          for (int n = 2 * hh; n < 2 * hh + 2; ++n)
            *(LAS float*)(uintptr_t)(((j & 1) ? hw1 : hw0) + (16 * m + 2 * j) * 256 + (n ^ (j & 2)) * 64) = acc[m][n][j];
    asm volatile("ds_write_b32 %0, %1" :: "v"(FLG_OFF + 16 + wq * 4), "v"(one) : "memory");
#pragma unroll
    for (int i = 0; i < 4; ++i) { hv[i][0] = *(const LAS f32x4*)(uintptr_t)(h0 + i * 4096); hv[i][1] = *(const LAS f32x4*)(uintptr_t)(h1 + i * 4096); }
    asm volatile(".Lcons_end:");
  }
  f32x4 av[2], bv[2];
#pragma unroll 1
  for (u32 sp = 0; sp < (1u << 22); ++sp) {
    u32 c;
    asm volatile("ds_read_b32 %0, %5\n\tds_read_b128 %1, %6\n\tds_read_b128 %2, %6 offset:16\n\tds_read_b128 %3, %6 offset:256\n\tds_read_b128 %4, %6 offset:272\n\ts_waitcnt lgkmcnt(0)"
                 : "=&v"(c), "=&v"(av[0]), "=&v"(av[1]), "=&v"(bv[0]), "=&v"(bv[1]) : "v"(abf), "v"(abo) : "memory");
    if ((u32)__builtin_amdgcn_readfirstlane(c) >= 2u) break;
  }
  const __amdgpu_buffer_rsrc_t orr = __builtin_amdgcn_make_buffer_rsrc((void*)outp, 0, (int)(xlen >> 1) - 640, 0x00020000);
  const float K1 = -2.0f * 0.7978845608028654f * 1.4426950408889634f;
  const float K3 = K1 * 0.044715f;
  const f32x2 K1v = {K1, K1}, K3v = {K3, K3}, ONE = {1.f, 1.f};
#pragma unroll
  for (int i = 0; i < 4; ++i) {
    f32x2 z[4], e[4];
#pragma unroll
    for (int j = 0; j < 4; ++j) {
      const f32x2 h2 = (j & 1) ? hv[i][j >> 1].zw : hv[i][j >> 1].xy;
      const f32x2 a2 = (j & 1) ? av[j >> 1].zw : av[j >> 1].xy;
      const f32x2 b2 = (j & 1) ? bv[j >> 1].zw : bv[j >> 1].xy;
      z[j] = __builtin_elementwise_fma(h2, a2, b2);
      e[j] = z[j] * __builtin_elementwise_fma(z[j] * z[j], K3v, K1v);
    }
#pragma unroll
    for (int j = 0; j < 4; ++j) { e[j].x = __builtin_amdgcn_exp2f(e[j].x); e[j].y = __builtin_amdgcn_exp2f(e[j].y); }
#pragma unroll
    for (int j = 0; j < 4; ++j) e[j] += ONE;
#pragma unroll
    for (int j = 0; j < 4; ++j) { e[j].x = __builtin_amdgcn_rcpf(e[j].x); e[j].y = __builtin_amdgcn_rcpf(e[j].y); }
    u32x4 o;
#pragma unroll
    for (int j = 0; j < 4; ++j) {
      const f32x2 y = z[j] * e[j];
      bf16x2 pk = {(__bf16)y.x, (__bf16)y.y};
      o[j] = __builtin_bit_cast(u32, pk);
    }
    __builtin_amdgcn_raw_buffer_store_b128(o, orr, ov + i * 16 * 512, 0, 16);
  }
  asm volatile(".Lkernel_end:");
}

extern "C" void kernel_launch(void* const* d_in, const int* in_sizes, int n_in, void* d_out, int out_size,
                              void* d_ws, size_t ws_size, hipStream_t stream) {
  (void)in_sizes; (void)n_in; (void)out_size; (void)ws_size;
  (void)hipFuncSetAttribute((const void*)conv_stem_kernel, hipFuncAttributeMaxDynamicSharedMemorySize, LDS_BYTES);
  hipLaunchKernelGGL(conv_stem_kernel, dim3(256), dim3(512), LDS_BYTES, stream, (const u16*)d_in[0], (const u16*)d_in[1],
                     (const u16*)d_in[2], (const u16*)d_in[3], (u16*)d_out, (char*)d_ws);
}
```

```cpp
#include <hip/hip_runtime.h>
#include <hip/hip_bf16.h>
#include <utility>

typedef unsigned short u16;
typedef unsigned int u32;
typedef unsigned long long u64;
typedef __attribute__((ext_vector_type(4))) short s16x4;
typedef __attribute__((ext_vector_type(8))) __bf16 bf16x8;
typedef __attribute__((ext_vector_type(2))) __bf16 bf16x2;
typedef __attribute__((ext_vector_type(4))) float f32x4;
typedef __attribute__((ext_vector_type(2))) float f32x2;
typedef __attribute__((ext_vector_type(4))) u32 u32x4;
typedef __attribute__((ext_vector_type(2))) u32 u32x2;
#define LAS __attribute__((address_space(3)))
#define GAS __attribute__((address_space(1)))

constexpr int T_ = 16000, L_ = 7999;
constexpr int TR = 250;
constexpr int SLOT = 48 * 1024;
constexpr int RED_OFF = 3 * SLOT;
constexpr int AB_OFF = RED_OFF + 2048;
constexpr int FLG_OFF = AB_OFF + 512;
constexpr int PF_OFF = FLG_OFF + 256;
constexpr int GB_OFF = PF_OFF + 1536;
constexpr int LDS_BYTES = GB_OFF + 256;
constexpr int WS_EPOCH = 0;
constexpr int WS_GRAN = 4096;
constexpr u32 OOB = 0x40000000u;
constexpr int POLL_DELAY0 = 22, POLL_DELAY1 = 18;
constexpr int G_ST = 52, G_W = 7;

#define SB() __builtin_amdgcn_sched_barrier(0)
#define PIN(v) asm volatile("" : "+v"(v))
#define WAIT_LGKM(n) __builtin_amdgcn_s_waitcnt(0xC07F | ((n) << 8))
__device__ __forceinline__ void bar() { asm volatile("" ::: "memory"); __builtin_amdgcn_s_barrier(); asm volatile("" ::: "memory"); }
__device__ __forceinline__ float bf2f(u32 v) { return __uint_as_float(v << 16); }
__device__ __forceinline__ bf16x8 cat(s16x4 a, s16x4 b) { return __builtin_bit_cast(bf16x8, __builtin_shufflevector(a, b, 0, 1, 2, 3, 4, 5, 6, 7)); }
__device__ __forceinline__ void lds_wait_ge(int a, u32 v) {
#pragma unroll 1
  for (u32 sp = 0; sp < (1u << 22); ++sp) {
    u32 c; asm volatile("ds_read_b32 %0, %1\n\ts_waitcnt lgkmcnt(0)" : "=v"(c) : "v"(a) : "memory");
    if ((u32)__builtin_amdgcn_readfirstlane(c) >= v) break;
    __builtin_amdgcn_s_sleep(1);
  }
}

#define TJ(e) ((e) < 4 ? (e) : ((e) == 4 ? 0 : ((e) < 7 ? (e) - 1 : (e) - 6)))
#define TH(e) ((e) == 4 || (e) >= 7 ? 1 : 0)
template <int... I, class F> __device__ __forceinline__ void sfor_(std::integer_sequence<int, I...>, F&& f) { (f(std::integral_constant<int, I>{}), ...); }
template <int N, class F> __device__ __forceinline__ void sfor(F&& f) { sfor_(std::make_integer_sequence<int, N>{}, f); }
#define CI(x) std::integral_constant<int, (x)>{}
struct Frags { bf16x8 a[4]; s16x4 b[4][2]; };

__global__ __launch_bounds__(512, 2) void conv_stem_kernel(const u16* __restrict__ x, const u16* __restrict__ w,
                                                           const u16* __restrict__ gam, const u16* __restrict__ bet,
                                                           u16* __restrict__ out, char* __restrict__ ws) {
  extern __shared__ __attribute__((aligned(16))) char smem_[];
  LAS char* smem = (LAS char*)smem_;
  asm volatile(".Lk_start:");
  const int bid = blockIdx.x;
  const int cg = (bid >> 3) & 3;
  const int rt = (bid & 7) * 8 + (bid >> 5);
  const int b = rt >> 5, t = rt & 31;
  const int l0 = t * TR;
  const int nvalid = (L_ - l0) < TR ? (L_ - l0) : TR;
  const int grp = b * 4 + cg;
  const int wid = __builtin_amdgcn_readfirstlane(threadIdx.x >> 6);
  const int lane = threadIdx.x & 63;
  const int wq = wid & 3;
  int h0, h1, abo, abf, ov;
  f32x4 hv[4][2];
  int xv = wq * 8192 + (lane >> 3) * 1024 + (((lane & 7) ^ (lane >> 4) ^ ((wq & 1) << 2)) << 4);
  int wv = wq * 4096 + (lane >> 3) * 512 + (((lane & 7) ^ (((lane >> 4) & 1) << 1) ^ ((wq & 1) << 2)) << 4);
  u32 xoff = (u32)(b * T_ + 2 * l0) * 512u, xlen = (u32)(2 * nvalid + 1) * 512u;
  const int pfi = (bid >> 5) * 48 + wq * 12 + lane;
  int pfv = lane < 12 ? ((pfi >> 7) * 256 + ((cg & 1) ^ 1) * 128 + (pfi & 127)) * 512 : (int)OOB;
  PIN(xv); PIN(wv); PIN(pfv);
  asm volatile("" : "+s"(xoff), "+s"(xlen));
  SB();
  asm volatile("" :: "s"(x), "s"(w), "s"(gam), "s"(bet), "s"(out), "s"(ws));
  u16* const outp = out + ((size_t)(b * L_ + l0) * 256 + cg * 64);
  auto epi_addr = [&]() {
    const int hf = wid < 4 ? 1 : 0;
    const int rr = lane >> 2, cc = lane & 3, sw = ((rr >> 1) & 1) | (((rr >> 2) & 1) << 3);
    const int hb = (wq >> 1) * 2 * SLOT + (wq & 1) * 16384 + rr * 256;
    h0 = hb + (((8 * hf + 2 * cc) ^ sw) << 4); h1 = h0 ^ 16;
    abo = AB_OFF + hf * 128 + cc * 32;
    abf = FLG_OFF + 8 + hf * 4;
    ov = (wq * 64 + rr) * 512 + hf * 64 + cc * 16;
    PIN(h0); PIN(h1); PIN(abo); PIN(abf); PIN(ov);
  };

  if (__builtin_expect(wid < 4, 1)) {
    const int lw = wq;
    __amdgpu_buffer_rsrc_t xr = __builtin_amdgcn_make_buffer_rsrc((void*)((const char*)x + xoff), 0, xlen, 0x00020000);
    __amdgpu_buffer_rsrc_t wr = __builtin_amdgcn_make_buffer_rsrc((void*)(w + cg * 64), 0, 768 * 512 - cg * 128, 0x00020000);
    const int rot = (cg & 1) * 2;
    LAS char* lbase = smem + lw * 1024;
    auto issue = [&](int u) {
      const int c = ((u >> 1) + rot) & 3, p = u & 1;
      LAS char* slot = lbase + (u % 3) * SLOT;
      if (p == 0) {
#pragma unroll
        for (int i = 0; i < 4; ++i)
          __builtin_amdgcn_raw_ptr_buffer_load_lds(wr, slot + 32768 + i * 4096, 16, wv, (i >> 1) * 262144 + c * 32768 + (i & 1) * 16384, 0, 0);
      } else {
#pragma unroll
        for (int i = 0; i < 2; ++i)
          __builtin_amdgcn_raw_ptr_buffer_load_lds(wr, slot + 32768 + i * 4096, 16, wv, 131072 + c * 32768 + i * 16384, 0, 0);
      }
#pragma unroll
      for (int i = 0; i < 8; ++i)
        __builtin_amdgcn_raw_ptr_buffer_load_lds(xr, slot + i * 4096, 16, xv, i * 32768 + p * 512 + c * 128, 0, 0);
    };
    issue(0);
    const __amdgpu_buffer_rsrc_t gr = __builtin_amdgcn_make_buffer_rsrc((void*)(ws + WS_GRAN + grp * 32768), 0, 32768, 0x00020000);
    const int ch = lw * 16 + (lane & 15);
#pragma unroll
    for (int k = 0; k < 8; ++k) {
      if (k + 1 < 8) {
        issue(k + 1);
        if ((k + 1) & 1) asm volatile("s_waitcnt vmcnt(10)" ::: "memory");
        else asm volatile("s_waitcnt vmcnt(12)" ::: "memory");
      } else {
        asm volatile("s_waitcnt vmcnt(0)" ::: "memory");
      }
      bar();
      if (k == 0) {
        __builtin_amdgcn_raw_ptr_buffer_load_lds(wr, smem + PF_OFF + lw * 256, 4, pfv, 0, 0, 0);
        if (lw == 1) {
          u64 pc; int c0, e1, e2;
          asm volatile("s_getpc_b64 %0\n\t.Lpf_here:\n\ts_mov_b32 %1, .Lk_start-.Lpf_here\n\ts_mov_b32 %2, .Lkernel_end-.Lpf_here\n\ts_mov_b32 %3, .Lcons_end-.Lpf_here"
                       : "=s"(pc), "=s"(c0), "=s"(e1), "=s"(e2));
          const int co = c0 + ((bid >> 3) * 4 + (lane & 3)) * 128;
          __builtin_amdgcn_global_load_lds((const GAS u32*)((const char*)pc + (co < (e1 > e2 ? e1 : e2) ? co : 0)), (LAS u32*)(smem + PF_OFF + 1024), 4, 0, 0);
        }
        if (lw == 2)
          __builtin_amdgcn_global_load_lds((const GAS u32*)((const char*)(lane < 32 ? gam : bet) + cg * 128 + (lane & 31) * 4), (LAS u32*)(smem + GB_OFF), 4, 0, 0);
        if (lw == 3 && lane < 2)
          __builtin_amdgcn_global_load_lds((const GAS u32*)((const char*)outp + lane * (248 * 512)), (LAS u32*)(smem + PF_OFF + 1280), 4, 0, 0);
        if (lw == 0 && lane < 2)
          __builtin_amdgcn_global_load_lds((const GAS u32*)(ws + WS_EPOCH + grp * 128 + lane * 4), (LAS u32*)(smem + FLG_OFF + 32), 4, 0, 16);
      }
    }
    u32 g_raw, b_raw; u32x2 tg;
    asm volatile("ds_read_u16 %0, %3\n\tds_read_u16 %1, %3 offset:128\n\tds_read_b64 %2, %4\n\ts_waitcnt lgkmcnt(0)"
                 : "=&v"(g_raw), "=&v"(b_raw), "=&v"(tg) : "v"(GB_OFF + ch * 2), "v"(FLG_OFF + 32) : "memory");
    const u32 tagA = __builtin_amdgcn_readfirstlane(tg.x) + 1u, tagB = __builtin_amdgcn_readfirstlane(tg.y) + 1u;
    epi_addr();
    float g_c = bf2f(g_raw), b_c = bf2f(b_raw);
    int abw = AB_OFF + (((lane >> 4) & 1) * 64 + ch) * 4, abc = FLG_OFF + 8 + (lw >> 1) * 4, one = 1;
    u32 voff = (lane >> 4) * 8192 + ch * 16;
    int hfl = FLG_OFF + 16 + lw * 4;
    PIN(g_c); PIN(b_c); PIN(abw); PIN(voff); PIN(abc); PIN(one); PIN(hfl);
    float s = 0.f, q = 0.f;
    lds_wait_ge(FLG_OFF + (lw >> 1) * 4, 4u);
    if (lw < 2) __builtin_amdgcn_s_sleep(POLL_DELAY0); else __builtin_amdgcn_s_sleep(POLL_DELAY1);
#pragma unroll 1
    for (u32 spin = 0; spin < (1u << 16); ++spin) {
      u32x4 gv[8];
      asm volatile(
          "buffer_load_dwordx4 %0, %8, %9, 0 offen sc1\n\t"
          "buffer_load_dwordx4 %1, %8, %9, 0 offen offset:1024 sc1\n\t"
          "buffer_load_dwordx4 %2, %8, %9, 0 offen offset:2048 sc1\n\t"
          "buffer_load_dwordx4 %3, %8, %9, 0 offen offset:3072 sc1\n\t"
          "buffer_load_dwordx4 %4, %8, %9, %10 offen sc1\n\t"
          "buffer_load_dwordx4 %5, %8, %9, %10 offen offset:1024 sc1\n\t"
          "buffer_load_dwordx4 %6, %8, %9, %10 offen offset:2048 sc1\n\t"
          "buffer_load_dwordx4 %7, %8, %9, %10 offen offset:3072 sc1\n\t"
          "s_waitcnt vmcnt(0)"
          : "=&v"(gv[0]), "=&v"(gv[1]), "=&v"(gv[2]), "=&v"(gv[3]), "=&v"(gv[4]), "=&v"(gv[5]), "=&v"(gv[6]), "=&v"(gv[7])
          : "v"(voff), "s"(gr), "s"(4096)
          : "memory");
      u32 m0 = voff & OOB, m1 = 0;
#pragma unroll
      for (int i = 0; i < 8; ++i) { m0 |= gv[i].y ^ tagA; m1 |= gv[i].w ^ tagB; }
      const float sn = ((__uint_as_float(gv[0].x) + __uint_as_float(gv[1].x)) + (__uint_as_float(gv[2].x) + __uint_as_float(gv[3].x))) +
                       ((__uint_as_float(gv[4].x) + __uint_as_float(gv[5].x)) + (__uint_as_float(gv[6].x) + __uint_as_float(gv[7].x)));
      const float qn = ((__uint_as_float(gv[0].z) + __uint_as_float(gv[1].z)) + (__uint_as_float(gv[2].z) + __uint_as_float(gv[3].z))) +
                       ((__uint_as_float(gv[4].z) + __uint_as_float(gv[5].z)) + (__uint_as_float(gv[6].z) + __uint_as_float(gv[7].z)));
      const bool ok = (m0 | m1) == 0u;
      s = ok ? sn : s; q = ok ? qn : q;
      voff = ok ? OOB : voff;
      if (__builtin_amdgcn_ballot_w64(voff != OOB) == 0ull) break;
      __builtin_amdgcn_s_sleep(1);
    }
    {
      auto r1 = __builtin_amdgcn_permlane32_swap(__float_as_uint(s), __float_as_uint(q), false, false);
      float u = __uint_as_float(r1[0]) + __uint_as_float(r1[1]);
      auto r2 = __builtin_amdgcn_permlane16_swap(__float_as_uint(u), __float_as_uint(u), false, false);
      u = __uint_as_float(r2[0]) + __uint_as_float(r2[1]);
      auto r3 = __builtin_amdgcn_permlane32_swap(__float_as_uint(u), __float_as_uint(u), false, false);
      s = __uint_as_float(r3[0]); q = __uint_as_float(r3[1]);
    }
    const float mu = s * (1.0f / L_);
    const float var = fmaxf(q * (1.0f / L_) - mu * mu, 0.f);
    const float a = g_c * __builtin_amdgcn_rsqf(var + 1e-5f);
    const float abv = (lane & 16) ? b_c - mu * a : a;
    asm volatile("ds_write_b32 %0, %1\n\ts_mov_b64 exec, 1\n\tds_add_u32 %2, %3\n\ts_mov_b64 exec, -1" :: "v"(abw), "v"(abv), "v"(abc), "v"(one) : "memory");
    if (t == 0 && lw == 0 && lane == 0)
      __hip_atomic_store((GAS u64*)(ws + WS_EPOCH + grp * 128), (u64)tagA | ((u64)tagB << 32), __ATOMIC_RELAXED, __HIP_MEMORY_SCOPE_AGENT);
#pragma unroll 1
    for (u32 sp = 0; sp < (1u << 22); ++sp) {
      u32 c;
      asm volatile("ds_read_b32 %0, %9\n\t"
                   "ds_read_b128 %1, %10\n\tds_read_b128 %2, %11\n\tds_read_b128 %3, %10 offset:4096\n\tds_read_b128 %4, %11 offset:4096\n\t"
                   "ds_read_b128 %5, %10 offset:8192\n\tds_read_b128 %6, %11 offset:8192\n\tds_read_b128 %7, %10 offset:12288\n\tds_read_b128 %8, %11 offset:12288\n\t"
                   "s_waitcnt lgkmcnt(0)"
                   : "=&v"(c), "=&v"(hv[0][0]), "=&v"(hv[0][1]), "=&v"(hv[1][0]), "=&v"(hv[1][1]), "=&v"(hv[2][0]), "=&v"(hv[2][1]), "=&v"(hv[3][0]), "=&v"(hv[3][1])
                   : "v"(hfl), "v"(h0), "v"(h1) : "memory");
      if (__builtin_amdgcn_readfirstlane(c) != 0) break;
      __builtin_amdgcn_s_sleep(2);
    }
  } else {
    char* gp = ws + WS_GRAN + grp * 32768;
    asm volatile("" : "+s"(gp));
    const __amdgpu_buffer_rsrc_t gr = __builtin_amdgcn_make_buffer_rsrc((void*)gp, 0, 32768, 0x00020000);
    if (wq == 0 && lane < 8) *(LAS u32*)(smem + FLG_OFF + lane * 4) = 0u;
    epi_addr();
    const int r = lane & 15, g = lane >> 4;
    const int rho = r < 4 ? 2 * r : (r >= 12 ? 2 * (r - 8) : 2 * (r - 4) + 1);
    int abase[2][2][2], abase3[2][2][2], bbase[2][4];
#pragma unroll
    for (int sh = 0; sh < 2; ++sh) {
      const int i = 64 * wq + rho + sh;
      const int i3 = (64 * wq + 48 + rho < nvalid) ? i + 48 : 254;
#pragma unroll
      for (int s = 0; s < 2; ++s) {
        abase[0][sh][s] = i * 128 + (((4 * s + g) ^ ((i >> 1) & 7)) << 4);
        abase3[0][sh][s] = i3 * 128 + (((4 * s + g) ^ ((i3 >> 1) & 7)) << 4);
        abase[1][sh][s] = abase[0][sh][s] + 2 * SLOT; abase3[1][sh][s] = abase3[0][sh][s] + 2 * SLOT;
        PIN(abase[0][sh][s]); PIN(abase3[0][sh][s]); PIN(abase[1][sh][s]); PIN(abase3[1][sh][s]);
      }
    }
    {
      const int q4 = (lane & 15) >> 2, p4 = lane & 3, sg = (q4 >> 1) + 2 * (g & 1);
#pragma unroll
      for (int n = 0; n < 4; ++n) {
        bbase[0][n] = 32768 + (8 * g + q4) * 128 + ((2 * (n ^ sg) + (p4 >> 1)) << 4) + 8 * (p4 & 1);
        bbase[1][n] = bbase[0][n] + 2 * SLOT;
        PIN(bbase[0][n]); PIN(bbase[1][n]);
      }
    }
    int hw0, hw1, redw = RED_OFF + wq * 512 + ((g >> 1) * 16 + r) * 8 + (g & 1) * 4, redo = RED_OFF + (lane & 31) * 8, flga = FLG_OFF, one = 1;
    u32 gvo = lane < 32 ? lane * 16 + t * 1024 : OOB, gvo1 = gvo + 512;
    int redo1 = redo + 256;
    PIN(gvo1); PIN(redo1);
    {
      const int rg = ((g & 1) ^ (g >> 1)) + 8 * (g >> 1);
      hw0 = (wq >> 1) * 2 * SLOT + (wq & 1) * 16384 + rg * 256 + r * 4;
      hw1 = hw0 ^ 16;
      PIN(hw0); PIN(hw1); PIN(redo); PIN(redw); PIN(gvo); PIN(flga); PIN(one);
    }
    u32 tagA = 0, tagB = 0;
    f32x4 acc[4][4];
#pragma unroll
    for (int m = 0; m < 4; ++m)
#pragma unroll
      for (int n = 0; n < 4; ++n) acc[m][n] = f32x4{0.f, 0.f, 0.f, 0.f};
    auto rdA = [&](int gs, int m) {
      const int pos = gs % 6, u = 2 * (gs / 6) + (pos >= 4 ? 1 : 0), ts = (pos == 2 || pos == 3) ? 1 : 0, s = pos & 1;
      const int hi = (u % 3) == 2, so = (u % 3) == 1 ? SLOT : 0;
      return m < 3 ? *(const LAS bf16x8*)(uintptr_t)(abase[hi][ts][s] + so + m * 2048) : *(const LAS bf16x8*)(uintptr_t)(abase3[hi][ts][s] + so);
    };
    auto rdB = [&](int gs, int n, int tt) {
      const int pos = gs % 6, u = 2 * (gs / 6) + (pos >= 4 ? 1 : 0), ts = (pos == 2 || pos == 3) ? 1 : 0, s = pos & 1;
      const int hi = (u % 3) == 2, so = (u % 3) == 1 ? SLOT : 0;
      return __builtin_amdgcn_ds_read_tr16_b64_v4i16((LAS s16x4*)(uintptr_t)(bbase[hi][n] + so + ts * 8192 + s * 4096 + tt * 512));
    };
    auto rd = [&](Frags& f, int gs, int idx) {
      if (idx == 0) f.a[0] = rdA(gs, 0);
      else if (idx <= 8) f.b[(idx - 1) >> 1][(idx - 1) & 1] = rdB(gs, (idx - 1) >> 1, (idx - 1) & 1);
      else f.a[idx - 8] = rdA(gs, idx - 8);
    };
    Frags tf[6];
    auto rdT = [&](auto kc) __attribute__((always_inline)) {
      constexpr int k = decltype(kc)::value;
      constexpr int e = k < 32 ? k >> 3 : (k < 36 ? 4 : (k < 52 ? 5 + ((k - 36) >> 3) : 7 + ((k - 52) >> 2)));
      constexpr int i = k < 32 ? k & 7 : (k < 36 ? k - 32 : (k < 52 ? (k - 36) & 7 : (k - 52) & 3));
      constexpr int j = TJ(e);
      if constexpr (TH(e)) tf[j].b[2 + (i >> 1)][i & 1] = rdB(18 + j, 2 + (i >> 1), i & 1);
      else if constexpr (i == 0) tf[j].a[0] = rdA(18 + j, 0);
      else if constexpr (i <= 4) tf[j].b[(i - 1) >> 1][(i - 1) & 1] = rdB(18 + j, (i - 1) >> 1, (i - 1) & 1);
      else tf[j].a[i - 4] = rdA(18 + j, i - 4);
    };
    f32x2 s2[4], q2[4];
    auto stat = [&](int m, int n, int part) {
      const f32x2 v = part ? acc[m][n].zw : acc[m][n].xy;
      if (m == 0 && part == 0) { s2[n] = v; q2[n] = v * v; }
      else { s2[n] += v; q2[n] = __builtin_elementwise_fma(v, v, q2[n]); }
    };
    auto red_half = [&](int h) {
      auto xs = __builtin_amdgcn_permlane32_swap(__float_as_uint(s2[2 * h].x + s2[2 * h].y), __float_as_uint(s2[2 * h + 1].x + s2[2 * h + 1].y), false, false);
      auto xq = __builtin_amdgcn_permlane32_swap(__float_as_uint(q2[2 * h].x + q2[2 * h].y), __float_as_uint(q2[2 * h + 1].x + q2[2 * h + 1].y), false, false);
      const float us = __uint_as_float(xs[0]) + __uint_as_float(xs[1]), uq = __uint_as_float(xq[0]) + __uint_as_float(xq[1]);
      auto y = __builtin_amdgcn_permlane16_swap(__float_as_uint(us), __float_as_uint(uq), false, false);
      return __uint_as_float(y[0]) + __uint_as_float(y[1]);
    };
    Frags fr[2];
    __builtin_amdgcn_s_waitcnt(0x0070);
    __builtin_amdgcn_s_setprio(1);
    bar();
#pragma unroll
    for (int i = 0; i < 12; ++i) { rd(fr[0], 0, i); SB(); }
#pragma unroll
    for (int gs = 0; gs < 17; ++gs) {
      Frags& cur = fr[gs & 1];
      Frags& nxt = fr[(gs + 1) & 1];
      const int np = (gs + 1) % 6;
      const bool newunit = np == 0 || np == 4;
      const int lead = newunit ? 4 : 1;
      WAIT_LGKM(3);
      SB();
#pragma unroll
      for (int i = 0; i < 16; ++i) {
        const int m = i >> 2, n = (m & 1) ? 3 - (i & 3) : (i & 3);
        if (i == 4 && !newunit) { WAIT_LGKM(4); SB(); }
        acc[m][n] = __builtin_amdgcn_mfma_f32_16x16x32_bf16(cur.a[m], cat(cur.b[n][0], cur.b[n][1]), acc[m][n], 0, 0, 0);
        SB();
        if (newunit && i == lead - 1) {
          WAIT_LGKM(0);
          bar();
          SB();
        }
        if (i >= lead - 1 && i < lead - 1 + 12) { rd(nxt, gs + 1, i - (lead - 1)); SB(); }
      }
    }
    WAIT_LGKM(3);
    SB();
    sfor<16>([&](auto ic) __attribute__((always_inline)) {
      constexpr int i = decltype(ic)::value, m = i >> 2, n = (m & 1) ? 3 - (i & 3) : (i & 3);
      acc[m][n] = __builtin_amdgcn_mfma_f32_16x16x32_bf16(fr[1].a[m], cat(fr[1].b[n][0], fr[1].b[n][1]), acc[m][n], 0, 0, 0);
      SB();
      if constexpr (i == 3) { WAIT_LGKM(0); bar(); SB(); }
      if constexpr (i >= 3) { rdT(CI(i - 3)); SB(); }
    });
    u32 cnt0; f32x4 ra, rb; u32x2 ep;
    sfor<96>([&](auto gc) __attribute__((always_inline)) {
      constexpr int gidx = decltype(gc)::value, e = gidx >> 3, i = gidx & 7, j = TJ(e), h = TH(e);
      constexpr int m = i >> 1, n = 2 * h + ((i ^ (i >> 1)) & 1);
      acc[m][n] = __builtin_amdgcn_mfma_f32_16x16x32_bf16(tf[j].a[m], cat(tf[j].b[n][0], tf[j].b[n][1]), acc[m][n], 0, 0, 0);
      SB();
      if constexpr (gidx < 23) { rdT(CI(13 + gidx)); SB(); }
      if constexpr (gidx == 23) { ep = *(const LAS u32x2*)(uintptr_t)(flga + 32); SB(); }
      if constexpr (gidx == 44) { tagA = ep.x + 1u; tagB = ep.y + 1u; PIN(tagA); PIN(tagB); SB(); }
      if constexpr (gidx == 24) { bar(); SB(); }
      if constexpr (gidx >= 24 && gidx < 60) { rdT(CI(36 + gidx - 24)); SB(); }
      if constexpr (gidx >= G_ST && gidx < G_ST + 8) { constexpr int k = gidx - G_ST; stat(k >> 1, (k ^ (k >> 1)) & 1, 0); stat(k >> 1, (k ^ (k >> 1)) & 1, 1); SB(); }
      if constexpr (gidx == G_ST + 9) {
        const float pr = red_half(0);
        asm volatile("ds_write_b32 %3, %4\n\t"
                     "s_mov_b64 exec, 1\n\t"
                     "ds_add_rtn_u32 %0, %5, %6\n\t"
                     "s_mov_b64 exec, -1\n\t"
                     "ds_read2st64_b64 %1, %7 offset1:1\n\t"
                     "ds_read2st64_b64 %2, %7 offset0:2 offset1:3"
                     : "=&v"(cnt0), "=&v"(ra), "=&v"(rb) : "v"(redw), "v"(pr), "v"(flga), "v"(one), "v"(redo) : "memory");
        SB();
      }
      if constexpr (gidx == G_ST + 9 + G_W) {
        asm volatile("s_waitcnt lgkmcnt(0)" : "+v"(cnt0), "+v"(ra), "+v"(rb) :: "memory");
        const f32x2 rr = (ra.xy + ra.zw) + (rb.xy + rb.zw);
        u32x4 gv = {__float_as_uint(rr.x), tagA, __float_as_uint(rr.y), tagB};
        __builtin_amdgcn_raw_buffer_store_b128(gv, gr, __builtin_amdgcn_readfirstlane(cnt0) == 3u ? gvo : OOB, 0, 16);
        SB();
      }
      if constexpr (gidx >= 92) { constexpr int k = 2 * (gidx - 92); stat(k >> 2, 2 + (((k >> 1) ^ (k >> 2)) & 1), 0); stat(k >> 2, 2 + (((k >> 1) ^ (k >> 2)) & 1), 1); SB(); }
    });
#pragma unroll
    for (int k = 8; k < 16; ++k) stat(k >> 2, 2 + (((k >> 1) ^ (k >> 2)) & 1), k & 1);
    {
      const float pr = red_half(1);
      u32 old1, c0; f32x4 r01, r23;
      asm volatile("ds_write_b32 %4, %5 offset:256\n\t"
                   "s_mov_b64 exec, 1\n\t"
                   "ds_add_rtn_u32 %0, %6, %7 offset:4\n\t"
                   "s_mov_b64 exec, -1\n\t"
                   "ds_read2st64_b64 %1, %8 offset1:1\n\t"
                   "ds_read2st64_b64 %2, %8 offset0:2 offset1:3\n\t"
                   "ds_read_b32 %3, %6\n\t"
                   "s_waitcnt lgkmcnt(0)"
                   : "=&v"(old1), "=&v"(r01), "=&v"(r23), "=&v"(c0) : "v"(redw), "v"(pr), "v"(flga), "v"(one), "v"(redo1) : "memory");
      const f32x2 rr = (r01.xy + r01.zw) + (r23.xy + r23.zw);
      u32x4 gv = {__float_as_uint(rr.x), tagA, __float_as_uint(rr.y), tagB};
      __builtin_amdgcn_raw_buffer_store_b128(gv, gr, __builtin_amdgcn_readfirstlane(old1) == 3u ? gvo1 : OOB, 0, 16);
      __builtin_amdgcn_s_setprio(0);
      if ((u32)__builtin_amdgcn_readfirstlane(c0) < 4u) lds_wait_ge(FLG_OFF, 4u);
    }
#pragma unroll
    for (int hh = 0; hh < 2; ++hh)
#pragma unroll
      for (int m = 0; m < 4; ++m)
#pragma unroll
        for (int j = 0; j < 4; ++j)
#pragma unroll
          for (int n = 2 * hh; n < 2 * hh + 2; ++n)
            *(LAS float*)(uintptr_t)(((j & 1) ? hw1 : hw0) + (16 * m + 2 * j) * 256 + (n ^ (j & 2)) * 64) = acc[m][n][j];
    asm volatile("ds_write_b32 %0, %1" :: "v"(FLG_OFF + 16 + wq * 4), "v"(one) : "memory");
#pragma unroll
    for (int i = 0; i < 4; ++i) { hv[i][0] = *(const LAS f32x4*)(uintptr_t)(h0 + i * 4096); hv[i][1] = *(const LAS f32x4*)(uintptr_t)(h1 + i * 4096); }
    asm volatile(".Lcons_end:");
  }
  f32x4 av[2], bv[2];
#pragma unroll 1
  for (u32 sp = 0; sp < (1u << 22); ++sp) {
    u32 c;
    asm volatile("ds_read_b32 %0, %5\n\tds_read_b128 %1, %6\n\tds_read_b128 %2, %6 offset:16\n\tds_read_b128 %3, %6 offset:256\n\tds_read_b128 %4, %6 offset:272\n\ts_waitcnt lgkmcnt(0)"
                 : "=&v"(c), "=&v"(av[0]), "=&v"(av[1]), "=&v"(bv[0]), "=&v"(bv[1]) : "v"(abf), "v"(abo) : "memory");
    if ((u32)__builtin_amdgcn_readfirstlane(c) >= 2u) break;
  }
  const __amdgpu_buffer_rsrc_t orr = __builtin_amdgcn_make_buffer_rsrc((void*)outp, 0, (int)(xlen >> 1) - 640, 0x00020000);
  const float K1 = -2.0f * 0.7978845608028654f * 1.4426950408889634f;
  const float K3 = K1 * 0.044715f;
  const f32x2 K1v = {K1, K1}, K3v = {K3, K3}, ONE = {1.f, 1.f};
#pragma unroll
  for (int i = 0; i < 4; ++i) {
    f32x2 z[4], e[4];
#pragma unroll
    for (int j = 0; j < 4; ++j) {
      const f32x2 h2 = (j & 1) ? hv[i][j >> 1].zw : hv[i][j >> 1].xy;
      const f32x2 a2 = (j & 1) ? av[j >> 1].zw : av[j >> 1].xy;
      const f32x2 b2 = (j & 1) ? bv[j >> 1].zw : bv[j >> 1].xy;
      z[j] = __builtin_elementwise_fma(h2, a2, b2);
      e[j] = z[j] * __builtin_elementwise_fma(z[j] * z[j], K3v, K1v);
    }
#pragma unroll
    for (int j = 0; j < 4; ++j) { e[j].x = __builtin_amdgcn_exp2f(e[j].x); e[j].y = __builtin_amdgcn_exp2f(e[j].y); }
#pragma unroll
    for (int j = 0; j < 4; ++j) e[j] += ONE;
#pragma unroll
    for (int j = 0; j < 4; ++j) { e[j].x = __builtin_amdgcn_rcpf(e[j].x); e[j].y = __builtin_amdgcn_rcpf(e[j].y); }
    u32x4 o;
#pragma unroll
    for (int j = 0; j < 4; ++j) {
      const f32x2 y = z[j] * e[j];
      bf16x2 pk = {(__bf16)y.x, (__bf16)y.y};
      o[j] = __builtin_bit_cast(u32, pk);
    }
    __builtin_amdgcn_raw_buffer_store_b128(o, orr, ov + i * 16 * 512, 0, 16);
  }
  asm volatile(".Lkernel_end:");
}

extern "C" void kernel_launch(void* const* d_in, const int* in_sizes, int n_in, void* d_out, int out_size,
                              void* d_ws, size_t ws_size, hipStream_t stream) {
  (void)in_sizes; (void)n_in; (void)out_size; (void)ws_size;
  (void)hipFuncSetAttribute((const void*)conv_stem_kernel, hipFuncAttributeMaxDynamicSharedMemorySize, LDS_BYTES);
  hipLaunchKernelGGL(conv_stem_kernel, dim3(256), dim3(512), LDS_BYTES, stream, (const u16*)d_in[0], (const u16*)d_in[1],
                     (const u16*)d_in[2], (const u16*)d_in[3], (u16*)d_out, (char*)d_ws);
}
```

```cpp
#include <hip/hip_runtime.h>
#include <hip/hip_bf16.h>
#include <utility>

typedef unsigned short u16;
typedef unsigned int u32;
typedef unsigned long long u64;
typedef __attribute__((ext_vector_type(4))) short s16x4;
typedef __attribute__((ext_vector_type(8))) __bf16 bf16x8;
typedef __attribute__((ext_vector_type(2))) __bf16 bf16x2;
typedef __attribute__((ext_vector_type(4))) float f32x4;
typedef __attribute__((ext_vector_type(2))) float f32x2;
typedef __attribute__((ext_vector_type(4))) u32 u32x4;
typedef __attribute__((ext_vector_type(2))) u32 u32x2;
#define LAS __attribute__((address_space(3)))
#define GAS __attribute__((address_space(1)))

constexpr int T_ = 16000, L_ = 7999;
constexpr int TR = 250;
constexpr int SLOT = 48 * 1024;
constexpr int RED_OFF = 3 * SLOT;
constexpr int AB_OFF = RED_OFF + 2048;
constexpr int FLG_OFF = AB_OFF + 512;
constexpr int PF_OFF = FLG_OFF + 256;
constexpr int GB_OFF = PF_OFF + 1536;
constexpr int LDS_BYTES = GB_OFF + 256;
constexpr int WS_EPOCH = 0;
constexpr int WS_GRAN = 4096;
constexpr u32 OOB = 0x40000000u;
constexpr int POLL_DELAY0 = 22, POLL_DELAY1 = 18;
constexpr int G_ST = 52, G_W = 7;

#define SB() __builtin_amdgcn_sched_barrier(0)
#define PIN(v) asm volatile("" : "+v"(v))
#define WAIT_LGKM(n) __builtin_amdgcn_s_waitcnt(0xC07F | ((n) << 8))
__device__ __forceinline__ void bar() { asm volatile("" ::: "memory"); __builtin_amdgcn_s_barrier(); asm volatile("" ::: "memory"); }
__device__ __forceinline__ float bf2f(u32 v) { return __uint_as_float(v << 16); }
__device__ __forceinline__ bf16x8 cat(s16x4 a, s16x4 b) { return __builtin_bit_cast(bf16x8, __builtin_shufflevector(a, b, 0, 1, 2, 3, 4, 5, 6, 7)); }
__device__ __forceinline__ void lds_wait_ge(int a, u32 v) {
#pragma unroll 1
  for (u32 sp = 0; sp < (1u << 24); ++sp) {
    u32 c; asm volatile("ds_read_b32 %0, %1\n\ts_waitcnt lgkmcnt(0)" : "=v"(c) : "v"(a) : "memory");
    if ((u32)__builtin_amdgcn_readfirstlane(c) >= v) break;
    __builtin_amdgcn_s_sleep(1);
  }
}

#define TJ(e) ((e) < 4 ? (e) : ((e) == 4 ? 0 : ((e) < 7 ? (e) - 1 : (e) - 6)))
#define TH(e) ((e) == 4 || (e) >= 7 ? 1 : 0)
template <int... I, class F> __device__ __forceinline__ void sfor_(std::integer_sequence<int, I...>, F&& f) { (f(std::integral_constant<int, I>{}), ...); }
template <int N, class F> __device__ __forceinline__ void sfor(F&& f) { sfor_(std::make_integer_sequence<int, N>{}, f); }
#define CI(x) std::integral_constant<int, (x)>{}
struct Frags { bf16x8 a[4]; s16x4 b[4][2]; };

__global__ __launch_bounds__(512, 2) void conv_stem_kernel(const u16* __restrict__ x, const u16* __restrict__ w,
                                                           const u16* __restrict__ gam, const u16* __restrict__ bet,
                                                           u16* __restrict__ out, char* __restrict__ ws) {
  extern __shared__ __attribute__((aligned(16))) char smem_[];
  LAS char* smem = (LAS char*)smem_;
  asm volatile(".Lk_start:");
  const u64 t_start = __builtin_amdgcn_s_memrealtime();
#define HOLD() do { while (__builtin_amdgcn_s_memrealtime() - t_start < 1600ull) __builtin_amdgcn_s_sleep(1); } while (0)
  const int bid = blockIdx.x;
  const int cg = (bid >> 3) & 3;
  const int rt = (bid & 7) * 8 + (bid >> 5);
  const int b = rt >> 5, t = rt & 31;
  const int l0 = t * TR;
  const int nvalid = (L_ - l0) < TR ? (L_ - l0) : TR;
  const int grp = b * 4 + cg;
  const int wid = __builtin_amdgcn_readfirstlane(threadIdx.x >> 6);
  const int lane = threadIdx.x & 63;
  const int wq = wid & 3;
  int h0, h1, abo, abf, ov;
  f32x4 hv[4][2];
  int xv = wq * 8192 + (lane >> 3) * 1024 + (((lane & 7) ^ (lane >> 4) ^ ((wq & 1) << 2)) << 4);
  int wv = wq * 4096 + (lane >> 3) * 512 + (((lane & 7) ^ (((lane >> 4) & 1) << 1) ^ ((wq & 1) << 2)) << 4);
  u32 xoff = (u32)(b * T_ + 2 * l0) * 512u, xlen = (u32)(2 * nvalid + 1) * 512u;
  const int pfi = (bid >> 5) * 48 + wq * 12 + lane;
  int pfv = lane < 12 ? ((pfi >> 7) * 256 + ((cg & 1) ^ 1) * 128 + (pfi & 127)) * 512 : (int)OOB;
  PIN(xv); PIN(wv); PIN(pfv);
  asm volatile("" : "+s"(xoff), "+s"(xlen));
  SB();
  asm volatile("" :: "s"(x), "s"(w), "s"(gam), "s"(bet), "s"(out), "s"(ws));
  u16* const outp = out + ((size_t)(b * L_ + l0) * 256 + cg * 64);
  auto epi_addr = [&]() {
    const int hf = wid < 4 ? 1 : 0;
    const int rr = lane >> 2, cc = lane & 3, sw = ((rr >> 1) & 1) | (((rr >> 2) & 1) << 3);
    const int hb = (wq >> 1) * 2 * SLOT + (wq & 1) * 16384 + rr * 256;
    h0 = hb + (((8 * hf + 2 * cc) ^ sw) << 4); h1 = h0 ^ 16;
    abo = AB_OFF + hf * 128 + cc * 32;
    abf = FLG_OFF + 8 + hf * 4;
    ov = (wq * 64 + rr) * 512 + hf * 64 + cc * 16;
    PIN(h0); PIN(h1); PIN(abo); PIN(abf); PIN(ov);
  };

  if (__builtin_expect(wid < 4, 1)) {
    const int lw = wq;
    __amdgpu_buffer_rsrc_t xr = __builtin_amdgcn_make_buffer_rsrc((void*)((const char*)x + xoff), 0, xlen, 0x00020000);
    __amdgpu_buffer_rsrc_t wr = __builtin_amdgcn_make_buffer_rsrc((void*)(w + cg * 64), 0, 768 * 512 - cg * 128, 0x00020000);
    const int rot = (cg & 1) * 2;
    LAS char* lbase = smem + lw * 1024;
    auto issue = [&](int u) {
      const int c = ((u >> 1) + rot) & 3, p = u & 1;
      LAS char* slot = lbase + (u % 3) * SLOT;
      if (p == 0) {
#pragma unroll
        for (int i = 0; i < 4; ++i)
          __builtin_amdgcn_raw_ptr_buffer_load_lds(wr, slot + 32768 + i * 4096, 16, wv, (i >> 1) * 262144 + c * 32768 + (i & 1) * 16384, 0, 0);
      } else {
#pragma unroll
        for (int i = 0; i < 2; ++i)
          __builtin_amdgcn_raw_ptr_buffer_load_lds(wr, slot + 32768 + i * 4096, 16, wv, 131072 + c * 32768 + i * 16384, 0, 0);
      }
#pragma unroll
      for (int i = 0; i < 8; ++i)
        __builtin_amdgcn_raw_ptr_buffer_load_lds(xr, slot + i * 4096, 16, xv, i * 32768 + p * 512 + c * 128, 0, 0);
    };
    issue(0);
    const __amdgpu_buffer_rsrc_t gr = __builtin_amdgcn_make_buffer_rsrc((void*)(ws + WS_GRAN + grp * 32768), 0, 32768, 0x00020000);
    const int ch = lw * 16 + (lane & 15);
#pragma unroll
    for (int k = 0; k < 8; ++k) {
      if (k + 1 < 8) {
        issue(k + 1);
        if ((k + 1) & 1) asm volatile("s_waitcnt vmcnt(10)" ::: "memory");
        else asm volatile("s_waitcnt vmcnt(12)" ::: "memory");
      } else {
        asm volatile("s_waitcnt vmcnt(0)" ::: "memory");
      }
      bar();
      if (k == 0) {
        __builtin_amdgcn_raw_ptr_buffer_load_lds(wr, smem + PF_OFF + lw * 256, 4, pfv, 0, 0, 0);
        if (lw == 1) {
          u64 pc; int c0, e1, e2;
          asm volatile("s_getpc_b64 %0\n\t.Lpf_here:\n\ts_mov_b32 %1, .Lk_start-.Lpf_here\n\ts_mov_b32 %2, .Lkernel_end-.Lpf_here\n\ts_mov_b32 %3, .Lcons_end-.Lpf_here"
                       : "=s"(pc), "=s"(c0), "=s"(e1), "=s"(e2));
          const int co = c0 + ((bid >> 3) * 4 + (lane & 3)) * 128;
          __builtin_amdgcn_global_load_lds((const GAS u32*)((const char*)pc + (co < (e1 > e2 ? e1 : e2) ? co : 0)), (LAS u32*)(smem + PF_OFF + 1024), 4, 0, 0);
        }
        if (lw == 2)
          __builtin_amdgcn_global_load_lds((const GAS u32*)((const char*)(lane < 32 ? gam : bet) + cg * 128 + (lane & 31) * 4), (LAS u32*)(smem + GB_OFF), 4, 0, 0);
        if (lw == 3 && lane < 2)
          __builtin_amdgcn_global_load_lds((const GAS u32*)((const char*)outp + lane * (248 * 512)), (LAS u32*)(smem + PF_OFF + 1280), 4, 0, 0);
        if (lw == 0 && lane < 2)
          __builtin_amdgcn_global_load_lds((const GAS u32*)(ws + WS_EPOCH + grp * 128 + lane * 4), (LAS u32*)(smem + FLG_OFF + 32), 4, 0, 16);
      }
    }
    u32 g_raw, b_raw; u32x2 tg;
    asm volatile("ds_read_u16 %0, %3\n\tds_read_u16 %1, %3 offset:128\n\tds_read_b64 %2, %4\n\ts_waitcnt lgkmcnt(0)"
                 : "=&v"(g_raw), "=&v"(b_raw), "=&v"(tg) : "v"(GB_OFF + ch * 2), "v"(FLG_OFF + 32) : "memory");
    const u32 tagA = __builtin_amdgcn_readfirstlane(tg.x) + 1u, tagB = __builtin_amdgcn_readfirstlane(tg.y) + 1u;
    epi_addr();
    float g_c = bf2f(g_raw), b_c = bf2f(b_raw);
    int abw = AB_OFF + (((lane >> 4) & 1) * 64 + ch) * 4, abc = FLG_OFF + 8 + (lw >> 1) * 4, one = 1;
    u32 voff = (lane >> 4) * 8192 + ch * 16;
    int hfl = FLG_OFF + 16 + lw * 4;
    PIN(g_c); PIN(b_c); PIN(abw); PIN(voff); PIN(abc); PIN(one); PIN(hfl);
    float s = 0.f, q = 0.f;
    lds_wait_ge(FLG_OFF + (lw >> 1) * 4, 4u);
    if (lw < 2) __builtin_amdgcn_s_sleep(POLL_DELAY0); else __builtin_amdgcn_s_sleep(POLL_DELAY1);
#pragma unroll 1
    for (u32 spin = 0; spin < (1u << 19); ++spin) {
      u32x4 gv[8];
      asm volatile(
          "buffer_load_dwordx4 %0, %8, %9, 0 offen sc1\n\t"
          "buffer_load_dwordx4 %1, %8, %9, 0 offen offset:1024 sc1\n\t"
          "buffer_load_dwordx4 %2, %8, %9, 0 offen offset:2048 sc1\n\t"
          "buffer_load_dwordx4 %3, %8, %9, 0 offen offset:3072 sc1\n\t"
          "buffer_load_dwordx4 %4, %8, %9, %10 offen sc1\n\t"
          "buffer_load_dwordx4 %5, %8, %9, %10 offen offset:1024 sc1\n\t"
          "buffer_load_dwordx4 %6, %8, %9, %10 offen offset:2048 sc1\n\t"
          "buffer_load_dwordx4 %7, %8, %9, %10 offen offset:3072 sc1\n\t"
          "s_waitcnt vmcnt(0)"
          : "=&v"(gv[0]), "=&v"(gv[1]), "=&v"(gv[2]), "=&v"(gv[3]), "=&v"(gv[4]), "=&v"(gv[5]), "=&v"(gv[6]), "=&v"(gv[7])
          : "v"(voff), "s"(gr), "s"(4096)
          : "memory");
      u32 m0 = voff & OOB, m1 = 0;
#pragma unroll
      for (int i = 0; i < 8; ++i) { m0 |= gv[i].y ^ tagA; m1 |= gv[i].w ^ tagB; }
      const float sn = ((__uint_as_float(gv[0].x) + __uint_as_float(gv[1].x)) + (__uint_as_float(gv[2].x) + __uint_as_float(gv[3].x))) +
                       ((__uint_as_float(gv[4].x) + __uint_as_float(gv[5].x)) + (__uint_as_float(gv[6].x) + __uint_as_float(gv[7].x)));
      const float qn = ((__uint_as_float(gv[0].z) + __uint_as_float(gv[1].z)) + (__uint_as_float(gv[2].z) + __uint_as_float(gv[3].z))) +
                       ((__uint_as_float(gv[4].z) + __uint_as_float(gv[5].z)) + (__uint_as_float(gv[6].z) + __uint_as_float(gv[7].z)));
      const bool ok = (m0 | m1) == 0u;
      s = ok ? sn : s; q = ok ? qn : q;
      voff = ok ? OOB : voff;
      if (__builtin_amdgcn_ballot_w64(voff != OOB) == 0ull) break;
      __builtin_amdgcn_s_sleep(1);
    }
    {
      auto r1 = __builtin_amdgcn_permlane32_swap(__float_as_uint(s), __float_as_uint(q), false, false);
      float u = __uint_as_float(r1[0]) + __uint_as_float(r1[1]);
      auto r2 = __builtin_amdgcn_permlane16_swap(__float_as_uint(u), __float_as_uint(u), false, false);
      u = __uint_as_float(r2[0]) + __uint_as_float(r2[1]);
      auto r3 = __builtin_amdgcn_permlane32_swap(__float_as_uint(u), __float_as_uint(u), false, false);
      s = __uint_as_float(r3[0]); q = __uint_as_float(r3[1]);
    }
    const float mu = s * (1.0f / L_);
    const float var = fmaxf(q * (1.0f / L_) - mu * mu, 0.f);
    const float a = g_c * __builtin_amdgcn_rsqf(var + 1e-5f);
    const float abv = (lane & 16) ? b_c - mu * a : a;
    asm volatile("ds_write_b32 %0, %1\n\ts_mov_b64 exec, 1\n\tds_add_u32 %2, %3\n\ts_mov_b64 exec, -1" :: "v"(abw), "v"(abv), "v"(abc), "v"(one) : "memory");
    if (t == 0 && lw == 0 && lane == 0)
      __hip_atomic_store((GAS u64*)(ws + WS_EPOCH + grp * 128), (u64)tagA | ((u64)tagB << 32), __ATOMIC_RELAXED, __HIP_MEMORY_SCOPE_AGENT);
#pragma unroll 1
    for (u32 sp = 0; sp < (1u << 24); ++sp) {
      u32 c;
      asm volatile("ds_read_b32 %0, %9\n\t"
                   "ds_read_b128 %1, %10\n\tds_read_b128 %2, %11\n\tds_read_b128 %3, %10 offset:4096\n\tds_read_b128 %4, %11 offset:4096\n\t"
                   "ds_read_b128 %5, %10 offset:8192\n\tds_read_b128 %6, %11 offset:8192\n\tds_read_b128 %7, %10 offset:12288\n\tds_read_b128 %8, %11 offset:12288\n\t"
                   "s_waitcnt lgkmcnt(0)"
                   : "=&v"(c), "=&v"(hv[0][0]), "=&v"(hv[0][1]), "=&v"(hv[1][0]), "=&v"(hv[1][1]), "=&v"(hv[2][0]), "=&v"(hv[2][1]), "=&v"(hv[3][0]), "=&v"(hv[3][1])
                   : "v"(hfl), "v"(h0), "v"(h1) : "memory");
      if (__builtin_amdgcn_readfirstlane(c) != 0) break;
      __builtin_amdgcn_s_sleep(2);
    }
  } else {
    char* gp = ws + WS_GRAN + grp * 32768;
    asm volatile("" : "+s"(gp));
    const __amdgpu_buffer_rsrc_t gr = __builtin_amdgcn_make_buffer_rsrc((void*)gp, 0, 32768, 0x00020000);
    if (wq == 0 && lane < 8) *(LAS u32*)(smem + FLG_OFF + lane * 4) = 0u;
    epi_addr();
    const int r = lane & 15, g = lane >> 4;
    const int rho = r < 4 ? 2 * r : (r >= 12 ? 2 * (r - 8) : 2 * (r - 4) + 1);
    int abase[2][2][2], abase3[2][2][2], bbase[2][4];
#pragma unroll
    for (int sh = 0; sh < 2; ++sh) {
      const int i = 64 * wq + rho + sh;
      const int i3 = (64 * wq + 48 + rho < nvalid) ? i + 48 : 254;
#pragma unroll
      for (int s = 0; s < 2; ++s) {
        abase[0][sh][s] = i * 128 + (((4 * s + g) ^ ((i >> 1) & 7)) << 4);
        abase3[0][sh][s] = i3 * 128 + (((4 * s + g) ^ ((i3 >> 1) & 7)) << 4);
        abase[1][sh][s] = abase[0][sh][s] + 2 * SLOT; abase3[1][sh][s] = abase3[0][sh][s] + 2 * SLOT;
        PIN(abase[0][sh][s]); PIN(abase3[0][sh][s]); PIN(abase[1][sh][s]); PIN(abase3[1][sh][s]);
      }
    }
    {
      const int q4 = (lane & 15) >> 2, p4 = lane & 3, sg = (q4 >> 1) + 2 * (g & 1);
#pragma unroll
      for (int n = 0; n < 4; ++n) {
        bbase[0][n] = 32768 + (8 * g + q4) * 128 + ((2 * (n ^ sg) + (p4 >> 1)) << 4) + 8 * (p4 & 1);
        bbase[1][n] = bbase[0][n] + 2 * SLOT;
        PIN(bbase[0][n]); PIN(bbase[1][n]);
      }
    }
    int hw0, hw1, redw = RED_OFF + wq * 512 + ((g >> 1) * 16 + r) * 8 + (g & 1) * 4, redo = RED_OFF + (lane & 31) * 8, flga = FLG_OFF, one = 1;
    u32 gvo = lane < 32 ? lane * 16 + t * 1024 : OOB, gvo1 = gvo + 512;
    int redo1 = redo + 256;
    PIN(gvo1); PIN(redo1);
    {
      const int rg = ((g & 1) ^ (g >> 1)) + 8 * (g >> 1);
      hw0 = (wq >> 1) * 2 * SLOT + (wq & 1) * 16384 + rg * 256 + r * 4;
      hw1 = hw0 ^ 16;
      PIN(hw0); PIN(hw1); PIN(redo); PIN(redw); PIN(gvo); PIN(flga); PIN(one);
    }
    u32 tagA = 0, tagB = 0;
    f32x4 acc[4][4];
#pragma unroll
    for (int m = 0; m < 4; ++m)
#pragma unroll
      for (int n = 0; n < 4; ++n) acc[m][n] = f32x4{0.f, 0.f, 0.f, 0.f};
    auto rdA = [&](int gs, int m) {
      const int pos = gs % 6, u = 2 * (gs / 6) + (pos >= 4 ? 1 : 0), ts = (pos == 2 || pos == 3) ? 1 : 0, s = pos & 1;
      const int hi = (u % 3) == 2, so = (u % 3) == 1 ? SLOT : 0;
      return m < 3 ? *(const LAS bf16x8*)(uintptr_t)(abase[hi][ts][s] + so + m * 2048) : *(const LAS bf16x8*)(uintptr_t)(abase3[hi][ts][s] + so);
    };
    auto rdB = [&](int gs, int n, int tt) {
      const int pos = gs % 6, u = 2 * (gs / 6) + (pos >= 4 ? 1 : 0), ts = (pos == 2 || pos == 3) ? 1 : 0, s = pos & 1;
      const int hi = (u % 3) == 2, so = (u % 3) == 1 ? SLOT : 0;
      return __builtin_amdgcn_ds_read_tr16_b64_v4i16((LAS s16x4*)(uintptr_t)(bbase[hi][n] + so + ts * 8192 + s * 4096 + tt * 512));
    };
    auto rd = [&](Frags& f, int gs, int idx) {
      if (idx == 0) f.a[0] = rdA(gs, 0);
      else if (idx <= 8) f.b[(idx - 1) >> 1][(idx - 1) & 1] = rdB(gs, (idx - 1) >> 1, (idx - 1) & 1);
      else f.a[idx - 8] = rdA(gs, idx - 8);
    };
    Frags tf[6];
    auto rdT = [&](auto kc) __attribute__((always_inline)) {
      constexpr int k = decltype(kc)::value;
      constexpr int e = k < 32 ? k >> 3 : (k < 36 ? 4 : (k < 52 ? 5 + ((k - 36) >> 3) : 7 + ((k - 52) >> 2)));
      constexpr int i = k < 32 ? k & 7 : (k < 36 ? k - 32 : (k < 52 ? (k - 36) & 7 : (k - 52) & 3));
      constexpr int j = TJ(e);
      if constexpr (TH(e)) tf[j].b[2 + (i >> 1)][i & 1] = rdB(18 + j, 2 + (i >> 1), i & 1);
      else if constexpr (i == 0) tf[j].a[0] = rdA(18 + j, 0);
      else if constexpr (i <= 4) tf[j].b[(i - 1) >> 1][(i - 1) & 1] = rdB(18 + j, (i - 1) >> 1, (i - 1) & 1);
      else tf[j].a[i - 4] = rdA(18 + j, i - 4);
    };
    f32x2 s2[4], q2[4];
    auto stat = [&](int m, int n, int part) {
      const f32x2 v = part ? acc[m][n].zw : acc[m][n].xy;
      if (m == 0 && part == 0) { s2[n] = v; q2[n] = v * v; }
      else { s2[n] += v; q2[n] = __builtin_elementwise_fma(v, v, q2[n]); }
    };
    auto red_half = [&](int h) {
      auto xs = __builtin_amdgcn_permlane32_swap(__float_as_uint(s2[2 * h].x + s2[2 * h].y), __float_as_uint(s2[2 * h + 1].x + s2[2 * h + 1].y), false, false);
      auto xq = __builtin_amdgcn_permlane32_swap(__float_as_uint(q2[2 * h].x + q2[2 * h].y), __float_as_uint(q2[2 * h + 1].x + q2[2 * h + 1].y), false, false);
      const float us = __uint_as_float(xs[0]) + __uint_as_float(xs[1]), uq = __uint_as_float(xq[0]) + __uint_as_float(xq[1]);
      auto y = __builtin_amdgcn_permlane16_swap(__float_as_uint(us), __float_as_uint(uq), false, false);
      return __uint_as_float(y[0]) + __uint_as_float(y[1]);
    };
    Frags fr[2];
    __builtin_amdgcn_s_waitcnt(0x0070);
    __builtin_amdgcn_s_setprio(1);
    bar();
#pragma unroll
    for (int i = 0; i < 12; ++i) { rd(fr[0], 0, i); SB(); }
#pragma unroll
    for (int gs = 0; gs < 17; ++gs) {
      Frags& cur = fr[gs & 1];
      Frags& nxt = fr[(gs + 1) & 1];
      const int np = (gs + 1) % 6;
      const bool newunit = np == 0 || np == 4;
      const int lead = newunit ? 4 : 1;
      WAIT_LGKM(3);
      SB();
#pragma unroll
      for (int i = 0; i < 16; ++i) {
        const int m = i >> 2, n = (m & 1) ? 3 - (i & 3) : (i & 3);
        if (i == 4 && !newunit) { WAIT_LGKM(4); SB(); }
        acc[m][n] = __builtin_amdgcn_mfma_f32_16x16x32_bf16(cur.a[m], cat(cur.b[n][0], cur.b[n][1]), acc[m][n], 0, 0, 0);
        SB();
        if (newunit && i == lead - 1) {
          WAIT_LGKM(0);
          bar();
          SB();
        }
        if (i >= lead - 1 && i < lead - 1 + 12) { rd(nxt, gs + 1, i - (lead - 1)); SB(); }
      }
    }
    WAIT_LGKM(3);
    SB();
    sfor<16>([&](auto ic) __attribute__((always_inline)) {
      constexpr int i = decltype(ic)::value, m = i >> 2, n = (m & 1) ? 3 - (i & 3) : (i & 3);
      acc[m][n] = __builtin_amdgcn_mfma_f32_16x16x32_bf16(fr[1].a[m], cat(fr[1].b[n][0], fr[1].b[n][1]), acc[m][n], 0, 0, 0);
      SB();
      if constexpr (i == 3) { WAIT_LGKM(0); bar(); SB(); }
      if constexpr (i >= 3) { rdT(CI(i - 3)); SB(); }
    });
    u32 cnt0; f32x4 ra, rb; u32x2 ep;
    sfor<96>([&](auto gc) __attribute__((always_inline)) {
      constexpr int gidx = decltype(gc)::value, e = gidx >> 3, i = gidx & 7, j = TJ(e), h = TH(e);
      constexpr int m = i >> 1, n = 2 * h + ((i ^ (i >> 1)) & 1);
      acc[m][n] = __builtin_amdgcn_mfma_f32_16x16x32_bf16(tf[j].a[m], cat(tf[j].b[n][0], tf[j].b[n][1]), acc[m][n], 0, 0, 0);
      SB();
      if constexpr (gidx < 23) { rdT(CI(13 + gidx)); SB(); }
      if constexpr (gidx == 23) { ep = *(const LAS u32x2*)(uintptr_t)(flga + 32); SB(); }
      if constexpr (gidx == 44) { tagA = ep.x + 1u; tagB = ep.y + 1u; PIN(tagA); PIN(tagB); SB(); }
      if constexpr (gidx == 24) { bar(); SB(); }
      if constexpr (gidx >= 24 && gidx < 60) { rdT(CI(36 + gidx - 24)); SB(); }
      if constexpr (gidx >= G_ST && gidx < G_ST + 8) { constexpr int k = gidx - G_ST; stat(k >> 1, (k ^ (k >> 1)) & 1, 0); stat(k >> 1, (k ^ (k >> 1)) & 1, 1); SB(); }
      if constexpr (gidx == G_ST + 9) {
        const float pr = red_half(0);
        asm volatile("ds_write_b32 %3, %4\n\t"
                     "s_mov_b64 exec, 1\n\t"
                     "ds_add_rtn_u32 %0, %5, %6\n\t"
                     "s_mov_b64 exec, -1\n\t"
                     "ds_read2st64_b64 %1, %7 offset1:1\n\t"
                     "ds_read2st64_b64 %2, %7 offset0:2 offset1:3"
                     : "=&v"(cnt0), "=&v"(ra), "=&v"(rb) : "v"(redw), "v"(pr), "v"(flga), "v"(one), "v"(redo) : "memory");
        SB();
      }
      if constexpr (gidx == G_ST + 9 + G_W) {
        asm volatile("s_waitcnt lgkmcnt(0)" : "+v"(cnt0), "+v"(ra), "+v"(rb) :: "memory");
        const f32x2 rr = (ra.xy + ra.zw) + (rb.xy + rb.zw);
        u32x4 gv = {__float_as_uint(rr.x), tagA, __float_as_uint(rr.y), tagB};
        __builtin_amdgcn_raw_buffer_store_b128(gv, gr, __builtin_amdgcn_readfirstlane(cnt0) == 3u ? gvo : OOB, 0, 16);
        SB();
      }
      if constexpr (gidx >= 92) { constexpr int k = 2 * (gidx - 92); stat(k >> 2, 2 + (((k >> 1) ^ (k >> 2)) & 1), 0); stat(k >> 2, 2 + (((k >> 1) ^ (k >> 2)) & 1), 1); SB(); }
    });
#pragma unroll
    for (int k = 8; k < 16; ++k) stat(k >> 2, 2 + (((k >> 1) ^ (k >> 2)) & 1), k & 1);
    {
      const float pr = red_half(1);
      u32 old1, c0; f32x4 r01, r23;
      asm volatile("ds_write_b32 %4, %5 offset:256\n\t"
                   "s_mov_b64 exec, 1\n\t"
                   "ds_add_rtn_u32 %0, %6, %7 offset:4\n\t"
                   "s_mov_b64 exec, -1\n\t"
                   "ds_read2st64_b64 %1, %8 offset1:1\n\t"
                   "ds_read2st64_b64 %2, %8 offset0:2 offset1:3\n\t"
                   "ds_read_b32 %3, %6\n\t"
                   "s_waitcnt lgkmcnt(0)"
                   : "=&v"(old1), "=&v"(r01), "=&v"(r23), "=&v"(c0) : "v"(redw), "v"(pr), "v"(flga), "v"(one), "v"(redo1) : "memory");
      const f32x2 rr = (r01.xy + r01.zw) + (r23.xy + r23.zw);
      u32x4 gv = {__float_as_uint(rr.x), tagA, __float_as_uint(rr.y), tagB};
      __builtin_amdgcn_raw_buffer_store_b128(gv, gr, __builtin_amdgcn_readfirstlane(old1) == 3u ? gvo1 : OOB, 0, 16);
      __builtin_amdgcn_s_setprio(0);
      if ((u32)__builtin_amdgcn_readfirstlane(c0) < 4u) lds_wait_ge(FLG_OFF, 4u);
    }
#pragma unroll
    for (int hh = 0; hh < 2; ++hh)
#pragma unroll
      for (int m = 0; m < 4; ++m)
#pragma unroll
        for (int j = 0; j < 4; ++j)
#pragma unroll
          for (int n = 2 * hh; n < 2 * hh + 2; ++n)
            *(LAS float*)(uintptr_t)(((j & 1) ? hw1 : hw0) + (16 * m + 2 * j) * 256 + (n ^ (j & 2)) * 64) = acc[m][n][j];
    asm volatile("ds_write_b32 %0, %1" :: "v"(FLG_OFF + 16 + wq * 4), "v"(one) : "memory");
#pragma unroll
    for (int i = 0; i < 4; ++i) { hv[i][0] = *(const LAS f32x4*)(uintptr_t)(h0 + i * 4096); hv[i][1] = *(const LAS f32x4*)(uintptr_t)(h1 + i * 4096); }
    asm volatile(".Lcons_end:");
  }
  f32x4 av[2], bv[2];
#pragma unroll 1
  for (u32 sp = 0; sp < (1u << 24); ++sp) {
    u32 c;
    asm volatile("ds_read_b32 %0, %5\n\tds_read_b128 %1, %6\n\tds_read_b128 %2, %6 offset:16\n\tds_read_b128 %3, %6 offset:256\n\tds_read_b128 %4, %6 offset:272\n\ts_waitcnt lgkmcnt(0)"
                 : "=&v"(c), "=&v"(av[0]), "=&v"(av[1]), "=&v"(bv[0]), "=&v"(bv[1]) : "v"(abf), "v"(abo) : "memory");
    if ((u32)__builtin_amdgcn_readfirstlane(c) >= 2u) break;
  }
  const __amdgpu_buffer_rsrc_t orr = __builtin_amdgcn_make_buffer_rsrc((void*)outp, 0, (int)(xlen >> 1) - 640, 0x00020000);
  const float K1 = -2.0f * 0.7978845608028654f * 1.4426950408889634f;
  const float K3 = K1 * 0.044715f;
  const f32x2 K1v = {K1, K1}, K3v = {K3, K3}, ONE = {1.f, 1.f};
#pragma unroll
  for (int i = 0; i < 4; ++i) {
    f32x2 z[4], e[4];
#pragma unroll
    for (int j = 0; j < 4; ++j) {
      const f32x2 h2 = (j & 1) ? hv[i][j >> 1].zw : hv[i][j >> 1].xy;
      const f32x2 a2 = (j & 1) ? av[j >> 1].zw : av[j >> 1].xy;
      const f32x2 b2 = (j & 1) ? bv[j >> 1].zw : bv[j >> 1].xy;
      z[j] = __builtin_elementwise_fma(h2, a2, b2);
      e[j] = z[j] * __builtin_elementwise_fma(z[j] * z[j], K3v, K1v);
    }
#pragma unroll
    for (int j = 0; j < 4; ++j) { e[j].x = __builtin_amdgcn_exp2f(e[j].x); e[j].y = __builtin_amdgcn_exp2f(e[j].y); }
#pragma unroll
    for (int j = 0; j < 4; ++j) e[j] += ONE;
#pragma unroll
    for (int j = 0; j < 4; ++j) { e[j].x = __builtin_amdgcn_rcpf(e[j].x); e[j].y = __builtin_amdgcn_rcpf(e[j].y); }
    u32x4 o;
#pragma unroll
    for (int j = 0; j < 4; ++j) {
      const f32x2 y = z[j] * e[j];
      bf16x2 pk = {(__bf16)y.x, (__bf16)y.y};
      o[j] = __builtin_bit_cast(u32, pk);
    }
    __builtin_amdgcn_raw_buffer_store_b128(o, orr, ov + i * 16 * 512, 0, 16);
  }
  asm volatile("s_waitcnt vmcnt(0)" ::: "memory");
  HOLD();
  asm volatile(".Lkernel_end:");
}

extern "C" void kernel_launch(void* const* d_in, const int* in_sizes, int n_in, void* d_out, int out_size,
                              void* d_ws, size_t ws_size, hipStream_t stream) {
  (void)in_sizes; (void)n_in; (void)out_size; (void)ws_size;
  (void)hipFuncSetAttribute((const void*)conv_stem_kernel, hipFuncAttributeMaxDynamicSharedMemorySize, LDS_BYTES);
  hipLaunchKernelGGL(conv_stem_kernel, dim3(256), dim3(512), LDS_BYTES, stream, (const u16*)d_in[0], (const u16*)d_in[1],
                     (const u16*)d_in[2], (const u16*)d_in[3], (u16*)d_out, (char*)d_ws);
}
```

```cpp
#include <hip/hip_runtime.h>
#include <hip/hip_bf16.h>
#include <utility>

typedef unsigned short u16;
typedef unsigned int u32;
typedef unsigned long long u64;
typedef __attribute__((ext_vector_type(4))) short s16x4;
typedef __attribute__((ext_vector_type(8))) __bf16 bf16x8;
typedef __attribute__((ext_vector_type(2))) __bf16 bf16x2;
typedef __attribute__((ext_vector_type(4))) float f32x4;
typedef __attribute__((ext_vector_type(2))) float f32x2;
typedef __attribute__((ext_vector_type(4))) u32 u32x4;
typedef __attribute__((ext_vector_type(2))) u32 u32x2;
#define LAS __attribute__((address_space(3)))
#define GAS __attribute__((address_space(1)))

constexpr int T_ = 16000, L_ = 7999;
constexpr int TR = 250;
constexpr int SLOT = 48 * 1024;
constexpr int RED_OFF = 3 * SLOT;
constexpr int AB_OFF = RED_OFF + 2048;
constexpr int FLG_OFF = AB_OFF + 512;
constexpr int PF_OFF = FLG_OFF + 256;
constexpr int GB_OFF = PF_OFF + 1024;
constexpr int LDS_BYTES = GB_OFF + 256;
constexpr int WS_EPOCH = 0;
constexpr int WS_GRAN = 4096;
constexpr u32 OOB = 0x40000000u;
constexpr int POLL_DELAY0 = 22, POLL_DELAY1 = 18;
constexpr int G_ST = 52, G_W = 7;

#define SB() __builtin_amdgcn_sched_barrier(0)
#define PIN(v) asm volatile("" : "+v"(v))
#define WAIT_LGKM(n) __builtin_amdgcn_s_waitcnt(0xC07F | ((n) << 8))
__device__ __forceinline__ void bar() { asm volatile("" ::: "memory"); __builtin_amdgcn_s_barrier(); asm volatile("" ::: "memory"); }
__device__ __forceinline__ float bf2f(u32 v) { return __uint_as_float(v << 16); }
__device__ __forceinline__ bf16x8 cat(s16x4 a, s16x4 b) { return __builtin_bit_cast(bf16x8, __builtin_shufflevector(a, b, 0, 1, 2, 3, 4, 5, 6, 7)); }
__device__ __forceinline__ void lds_wait_ge(int a, u32 v) {
#pragma unroll 1
  for (u32 sp = 0; sp < (1u << 24); ++sp) {
    u32 c; asm volatile("ds_read_b32 %0, %1\n\ts_waitcnt lgkmcnt(0)" : "=v"(c) : "v"(a) : "memory");
    if ((u32)__builtin_amdgcn_readfirstlane(c) >= v) break;
    __builtin_amdgcn_s_sleep(1);
  }
}

#define TJ(e) ((e) < 4 ? (e) : ((e) == 4 ? 0 : ((e) < 7 ? (e) - 1 : (e) - 6)))
#define TH(e) ((e) == 4 || (e) >= 7 ? 1 : 0)
template <int... I, class F> __device__ __forceinline__ void sfor_(std::integer_sequence<int, I...>, F&& f) { (f(std::integral_constant<int, I>{}), ...); }
template <int N, class F> __device__ __forceinline__ void sfor(F&& f) { sfor_(std::make_integer_sequence<int, N>{}, f); }
#define CI(x) std::integral_constant<int, (x)>{}
struct Frags { bf16x8 a[4]; s16x4 b[4][2]; };

__global__ __launch_bounds__(512, 2) void conv_stem_kernel(const u16* __restrict__ x, const u16* __restrict__ w,
                                                           const u16* __restrict__ gam, const u16* __restrict__ bet,
                                                           u16* __restrict__ out, char* __restrict__ ws) {
  extern __shared__ __attribute__((aligned(16))) char smem_[];
  LAS char* smem = (LAS char*)smem_;
  const int bid = blockIdx.x;
  const int cg = (bid >> 3) & 3;
  const int rt = (bid & 7) * 8 + (bid >> 5);
  const int b = rt >> 5, t = rt & 31;
  const int l0 = t * TR;
  const int nvalid = (L_ - l0) < TR ? (L_ - l0) : TR;
  const int grp = b * 4 + cg;
  const int wid = __builtin_amdgcn_readfirstlane(threadIdx.x >> 6);
  const int lane = threadIdx.x & 63;
  const int wq = wid & 3;
  int h0, h1, abo, abf, ov;
  f32x4 hv[4][2];
  int xv = wq * 8192 + (lane >> 3) * 1024 + (((lane & 7) ^ (lane >> 4) ^ ((wq & 1) << 2)) << 4);
  int wv = wq * 4096 + (lane >> 3) * 512 + (((lane & 7) ^ (((lane >> 4) & 1) << 1) ^ ((wq & 1) << 2)) << 4);
  u32 xoff = (u32)(b * T_ + 2 * l0) * 512u, xlen = (u32)(2 * nvalid + 1) * 512u;
  const int pfi = (bid >> 5) * 48 + wq * 12 + lane;
  int pfv = lane < 12 ? ((pfi >> 7) * 256 + ((cg & 1) ^ 1) * 128 + (pfi & 127)) * 512 : (int)OOB;
  PIN(xv); PIN(wv); PIN(pfv);
  asm volatile("" : "+s"(xoff), "+s"(xlen));
  SB();
  asm volatile("" :: "s"(x), "s"(w), "s"(gam), "s"(bet), "s"(out), "s"(ws));
  u16* const outp = out + ((size_t)(b * L_ + l0) * 256 + cg * 64);
  auto epi_addr = [&]() {
    const int hf = wid < 4 ? 1 : 0;
    const int rr = lane >> 2, cc = lane & 3, sw = ((rr >> 1) & 1) | (((rr >> 2) & 1) << 3);
    const int hb = (wq >> 1) * 2 * SLOT + (wq & 1) * 16384 + rr * 256;
    h0 = hb + (((8 * hf + 2 * cc) ^ sw) << 4); h1 = h0 ^ 16;
    abo = AB_OFF + hf * 128 + cc * 32;
    abf = FLG_OFF + 8 + hf * 4;
    ov = (wq * 64 + rr) * 512 + hf * 64 + cc * 16;
    PIN(h0); PIN(h1); PIN(abo); PIN(abf); PIN(ov);
  };

  if (__builtin_expect(wid < 4, 1)) {
    const int lw = wq;
    __amdgpu_buffer_rsrc_t xr = __builtin_amdgcn_make_buffer_rsrc((void*)((const char*)x + xoff), 0, xlen, 0x00020000);
    __amdgpu_buffer_rsrc_t wr = __builtin_amdgcn_make_buffer_rsrc((void*)(w + cg * 64), 0, 768 * 512 - cg * 128, 0x00020000);
    const int rot = (cg & 1) * 2;
    LAS char* lbase = smem + lw * 1024;
    auto issue = [&](int u) {
      const int c = ((u >> 1) + rot) & 3, p = u & 1;
      LAS char* slot = lbase + (u % 3) * SLOT;
      if (p == 0) {
#pragma unroll
        for (int i = 0; i < 4; ++i)
          __builtin_amdgcn_raw_ptr_buffer_load_lds(wr, slot + 32768 + i * 4096, 16, wv, (i >> 1) * 262144 + c * 32768 + (i & 1) * 16384, 0, 0);
      } else {
#pragma unroll
        for (int i = 0; i < 2; ++i)
          __builtin_amdgcn_raw_ptr_buffer_load_lds(wr, slot + 32768 + i * 4096, 16, wv, 131072 + c * 32768 + i * 16384, 0, 0);
      }
#pragma unroll
      for (int i = 0; i < 8; ++i)
        __builtin_amdgcn_raw_ptr_buffer_load_lds(xr, slot + i * 4096, 16, xv, i * 32768 + p * 512 + c * 128, 0, 0);
    };
    issue(0);
    const __amdgpu_buffer_rsrc_t gr = __builtin_amdgcn_make_buffer_rsrc((void*)(ws + WS_GRAN + grp * 32768), 0, 32768, 0x00020000);
    const int ch = lw * 16 + (lane & 15);
#pragma unroll
    for (int k = 0; k < 8; ++k) {
      if (k + 1 < 8) {
        issue(k + 1);
        if ((k + 1) & 1) asm volatile("s_waitcnt vmcnt(10)" ::: "memory");
        else asm volatile("s_waitcnt vmcnt(12)" ::: "memory");
      } else {
        asm volatile("s_waitcnt vmcnt(0)" ::: "memory");
      }
      bar();
      if (k == 0) {
        __builtin_amdgcn_raw_ptr_buffer_load_lds(wr, smem + PF_OFF + lw * 256, 4, pfv, 0, 0, 0);
        if (lw == 2)
          __builtin_amdgcn_global_load_lds((const GAS u32*)((const char*)(lane < 32 ? gam : bet) + cg * 128 + (lane & 31) * 4), (LAS u32*)(smem + GB_OFF), 4, 0, 0);
        if (lw == 0 && lane < 2)
          __builtin_amdgcn_global_load_lds((const GAS u32*)(ws + WS_EPOCH + grp * 128 + lane * 4), (LAS u32*)(smem + FLG_OFF + 32), 4, 0, 16);
      }
    }
    u32 g_raw, b_raw; u32x2 tg;
    asm volatile("ds_read_u16 %0, %3\n\tds_read_u16 %1, %3 offset:128\n\tds_read_b64 %2, %4\n\ts_waitcnt lgkmcnt(0)"
                 : "=&v"(g_raw), "=&v"(b_raw), "=&v"(tg) : "v"(GB_OFF + ch * 2), "v"(FLG_OFF + 32) : "memory");
    const u32 tagA = __builtin_amdgcn_readfirstlane(tg.x) + 1u, tagB = __builtin_amdgcn_readfirstlane(tg.y) + 1u;
    epi_addr();
    float g_c = bf2f(g_raw), b_c = bf2f(b_raw);
    int abw = AB_OFF + (((lane >> 4) & 1) * 64 + ch) * 4, abc = FLG_OFF + 8 + (lw >> 1) * 4, one = 1;
    u32 voff = (lane >> 4) * 8192 + ch * 16;
    int hfl = FLG_OFF + 16 + lw * 4;
    PIN(g_c); PIN(b_c); PIN(abw); PIN(voff); PIN(abc); PIN(one); PIN(hfl);
    float s = 0.f, q = 0.f;
    lds_wait_ge(FLG_OFF + (lw >> 1) * 4, 4u);
    if (lw < 2) __builtin_amdgcn_s_sleep(POLL_DELAY0); else __builtin_amdgcn_s_sleep(POLL_DELAY1);
#pragma unroll 1
    for (u32 spin = 0; spin < (1u << 19); ++spin) {
      u32x4 gv[8];
      asm volatile(
          "buffer_load_dwordx4 %0, %8, %9, 0 offen sc1\n\t"
          "buffer_load_dwordx4 %1, %8, %9, 0 offen offset:1024 sc1\n\t"
          "buffer_load_dwordx4 %2, %8, %9, 0 offen offset:2048 sc1\n\t"
          "buffer_load_dwordx4 %3, %8, %9, 0 offen offset:3072 sc1\n\t"
          "buffer_load_dwordx4 %4, %8, %9, %10 offen sc1\n\t"
          "buffer_load_dwordx4 %5, %8, %9, %10 offen offset:1024 sc1\n\t"
          "buffer_load_dwordx4 %6, %8, %9, %10 offen offset:2048 sc1\n\t"
          "buffer_load_dwordx4 %7, %8, %9, %10 offen offset:3072 sc1\n\t"
          "s_waitcnt vmcnt(0)"
          : "=&v"(gv[0]), "=&v"(gv[1]), "=&v"(gv[2]), "=&v"(gv[3]), "=&v"(gv[4]), "=&v"(gv[5]), "=&v"(gv[6]), "=&v"(gv[7])
          : "v"(voff), "s"(gr), "s"(4096)
          : "memory");
      u32 m0 = voff & OOB, m1 = 0;
#pragma unroll
      for (int i = 0; i < 8; ++i) { m0 |= gv[i].y ^ tagA; m1 |= gv[i].w ^ tagB; }
      const float sn = ((__uint_as_float(gv[0].x) + __uint_as_float(gv[1].x)) + (__uint_as_float(gv[2].x) + __uint_as_float(gv[3].x))) +
                       ((__uint_as_float(gv[4].x) + __uint_as_float(gv[5].x)) + (__uint_as_float(gv[6].x) + __uint_as_float(gv[7].x)));
      const float qn = ((__uint_as_float(gv[0].z) + __uint_as_float(gv[1].z)) + (__uint_as_float(gv[2].z) + __uint_as_float(gv[3].z))) +
                       ((__uint_as_float(gv[4].z) + __uint_as_float(gv[5].z)) + (__uint_as_float(gv[6].z) + __uint_as_float(gv[7].z)));
      const bool ok = (m0 | m1) == 0u;
      s = ok ? sn : s; q = ok ? qn : q;
      voff = ok ? OOB : voff;
      if (__builtin_amdgcn_ballot_w64(voff != OOB) == 0ull) break;
      __builtin_amdgcn_s_sleep(1);
    }
    {
      auto r1 = __builtin_amdgcn_permlane32_swap(__float_as_uint(s), __float_as_uint(q), false, false);
      float u = __uint_as_float(r1[0]) + __uint_as_float(r1[1]);
      auto r2 = __builtin_amdgcn_permlane16_swap(__float_as_uint(u), __float_as_uint(u), false, false);
      u = __uint_as_float(r2[0]) + __uint_as_float(r2[1]);
      auto r3 = __builtin_amdgcn_permlane32_swap(__float_as_uint(u), __float_as_uint(u), false, false);
      s = __uint_as_float(r3[0]); q = __uint_as_float(r3[1]);
    }
    const float mu = s * (1.0f / L_);
    const float var = fmaxf(q * (1.0f / L_) - mu * mu, 0.f);
    const float a = g_c * __builtin_amdgcn_rsqf(var + 1e-5f);
    const float abv = (lane & 16) ? b_c - mu * a : a;
    asm volatile("ds_write_b32 %0, %1\n\ts_mov_b64 exec, 1\n\tds_add_u32 %2, %3\n\ts_mov_b64 exec, -1" :: "v"(abw), "v"(abv), "v"(abc), "v"(one) : "memory");
    if (t == 0 && lw == 0 && lane == 0)
      __hip_atomic_store((GAS u64*)(ws + WS_EPOCH + grp * 128), (u64)tagA | ((u64)tagB << 32), __ATOMIC_RELAXED, __HIP_MEMORY_SCOPE_AGENT);
#pragma unroll 1
    for (u32 sp = 0; sp < (1u << 24); ++sp) {
      u32 c;
      asm volatile("ds_read_b32 %0, %9\n\t"
                   "ds_read_b128 %1, %10\n\tds_read_b128 %2, %11\n\tds_read_b128 %3, %10 offset:4096\n\tds_read_b128 %4, %11 offset:4096\n\t"
                   "ds_read_b128 %5, %10 offset:8192\n\tds_read_b128 %6, %11 offset:8192\n\tds_read_b128 %7, %10 offset:12288\n\tds_read_b128 %8, %11 offset:12288\n\t"
                   "s_waitcnt lgkmcnt(0)"
                   : "=&v"(c), "=&v"(hv[0][0]), "=&v"(hv[0][1]), "=&v"(hv[1][0]), "=&v"(hv[1][1]), "=&v"(hv[2][0]), "=&v"(hv[2][1]), "=&v"(hv[3][0]), "=&v"(hv[3][1])
                   : "v"(hfl), "v"(h0), "v"(h1) : "memory");
      if (__builtin_amdgcn_readfirstlane(c) != 0) break;
      __builtin_amdgcn_s_sleep(2);
    }
  } else {
    char* gp = ws + WS_GRAN + grp * 32768;
    asm volatile("" : "+s"(gp));
    const __amdgpu_buffer_rsrc_t gr = __builtin_amdgcn_make_buffer_rsrc((void*)gp, 0, 32768, 0x00020000);
    if (wq == 0 && lane < 8) *(LAS u32*)(smem + FLG_OFF + lane * 4) = 0u;
    epi_addr();
    const int r = lane & 15, g = lane >> 4;
    const int rho = r < 4 ? 2 * r : (r >= 12 ? 2 * (r - 8) : 2 * (r - 4) + 1);
    int abase[2][2][2], abase3[2][2][2], bbase[2][4];
#pragma unroll
    for (int sh = 0; sh < 2; ++sh) {
      const int i = 64 * wq + rho + sh;
      const int i3 = (64 * wq + 48 + rho < nvalid) ? i + 48 : 254;
#pragma unroll
      for (int s = 0; s < 2; ++s) {
        abase[0][sh][s] = i * 128 + (((4 * s + g) ^ ((i >> 1) & 7)) << 4);
        abase3[0][sh][s] = i3 * 128 + (((4 * s + g) ^ ((i3 >> 1) & 7)) << 4);
        abase[1][sh][s] = abase[0][sh][s] + 2 * SLOT; abase3[1][sh][s] = abase3[0][sh][s] + 2 * SLOT;
        PIN(abase[0][sh][s]); PIN(abase3[0][sh][s]); PIN(abase[1][sh][s]); PIN(abase3[1][sh][s]);
      }
    }
    {
      const int q4 = (lane & 15) >> 2, p4 = lane & 3, sg = (q4 >> 1) + 2 * (g & 1);
#pragma unroll
      for (int n = 0; n < 4; ++n) {
        bbase[0][n] = 32768 + (8 * g + q4) * 128 + ((2 * (n ^ sg) + (p4 >> 1)) << 4) + 8 * (p4 & 1);
        bbase[1][n] = bbase[0][n] + 2 * SLOT;
        PIN(bbase[0][n]); PIN(bbase[1][n]);
      }
    }
    int hw0, hw1, redw = RED_OFF + wq * 512 + ((g >> 1) * 16 + r) * 8 + (g & 1) * 4, redo = RED_OFF + (lane & 31) * 8, flga = FLG_OFF, one = 1;
    u32 gvo = lane < 32 ? lane * 16 + t * 1024 : OOB, gvo1 = gvo + 512;
    int redo1 = redo + 256;
    PIN(gvo1); PIN(redo1);
    {
      const int rg = ((g & 1) ^ (g >> 1)) + 8 * (g >> 1);
      hw0 = (wq >> 1) * 2 * SLOT + (wq & 1) * 16384 + rg * 256 + r * 4;
      hw1 = hw0 ^ 16;
      PIN(hw0); PIN(hw1); PIN(redo); PIN(redw); PIN(gvo); PIN(flga); PIN(one);
    }
    u32 tagA = 0, tagB = 0;
    f32x4 acc[4][4];
#pragma unroll
    for (int m = 0; m < 4; ++m)
#pragma unroll
      for (int n = 0; n < 4; ++n) acc[m][n] = f32x4{0.f, 0.f, 0.f, 0.f};
    auto rdA = [&](int gs, int m) {
      const int pos = gs % 6, u = 2 * (gs / 6) + (pos >= 4 ? 1 : 0), ts = (pos == 2 || pos == 3) ? 1 : 0, s = pos & 1;
      const int hi = (u % 3) == 2, so = (u % 3) == 1 ? SLOT : 0;
      return m < 3 ? *(const LAS bf16x8*)(uintptr_t)(abase[hi][ts][s] + so + m * 2048) : *(const LAS bf16x8*)(uintptr_t)(abase3[hi][ts][s] + so);
    };
    auto rdB = [&](int gs, int n, int tt) {
      const int pos = gs % 6, u = 2 * (gs / 6) + (pos >= 4 ? 1 : 0), ts = (pos == 2 || pos == 3) ? 1 : 0, s = pos & 1;
      const int hi = (u % 3) == 2, so = (u % 3) == 1 ? SLOT : 0;
      return __builtin_amdgcn_ds_read_tr16_b64_v4i16((LAS s16x4*)(uintptr_t)(bbase[hi][n] + so + ts * 8192 + s * 4096 + tt * 512));
    };
    auto rd = [&](Frags& f, int gs, int idx) {
      if (idx == 0) f.a[0] = rdA(gs, 0);
      else if (idx <= 8) f.b[(idx - 1) >> 1][(idx - 1) & 1] = rdB(gs, (idx - 1) >> 1, (idx - 1) & 1);
      else f.a[idx - 8] = rdA(gs, idx - 8);
    };
    Frags tf[6];
    auto rdT = [&](auto kc) __attribute__((always_inline)) {
      constexpr int k = decltype(kc)::value;
      constexpr int e = k < 32 ? k >> 3 : (k < 36 ? 4 : (k < 52 ? 5 + ((k - 36) >> 3) : 7 + ((k - 52) >> 2)));
      constexpr int i = k < 32 ? k & 7 : (k < 36 ? k - 32 : (k < 52 ? (k - 36) & 7 : (k - 52) & 3));
      constexpr int j = TJ(e);
      if constexpr (TH(e)) tf[j].b[2 + (i >> 1)][i & 1] = rdB(18 + j, 2 + (i >> 1), i & 1);
      else if constexpr (i == 0) tf[j].a[0] = rdA(18 + j, 0);
      else if constexpr (i <= 4) tf[j].b[(i - 1) >> 1][(i - 1) & 1] = rdB(18 + j, (i - 1) >> 1, (i - 1) & 1);
      else tf[j].a[i - 4] = rdA(18 + j, i - 4);
    };
    f32x2 s2[4], q2[4];
    auto stat = [&](int m, int n, int part) {
      const f32x2 v = part ? acc[m][n].zw : acc[m][n].xy;
      if (m == 0 && part == 0) { s2[n] = v; q2[n] = v * v; }
      else { s2[n] += v; q2[n] = __builtin_elementwise_fma(v, v, q2[n]); }
    };
    auto red_half = [&](int h) {
      auto xs = __builtin_amdgcn_permlane32_swap(__float_as_uint(s2[2 * h].x + s2[2 * h].y), __float_as_uint(s2[2 * h + 1].x + s2[2 * h + 1].y), false, false);
      auto xq = __builtin_amdgcn_permlane32_swap(__float_as_uint(q2[2 * h].x + q2[2 * h].y), __float_as_uint(q2[2 * h + 1].x + q2[2 * h + 1].y), false, false);
      const float us = __uint_as_float(xs[0]) + __uint_as_float(xs[1]), uq = __uint_as_float(xq[0]) + __uint_as_float(xq[1]);
      auto y = __builtin_amdgcn_permlane16_swap(__float_as_uint(us), __float_as_uint(uq), false, false);
      return __uint_as_float(y[0]) + __uint_as_float(y[1]);
    };
    Frags fr[2];
    __builtin_amdgcn_s_waitcnt(0x0070);
    __builtin_amdgcn_s_setprio(1);
    bar();
#pragma unroll
    for (int i = 0; i < 12; ++i) { rd(fr[0], 0, i); SB(); }
#pragma unroll
    for (int gs = 0; gs < 17; ++gs) {
      Frags& cur = fr[gs & 1];
      Frags& nxt = fr[(gs + 1) & 1];
      const int np = (gs + 1) % 6;
      const bool newunit = np == 0 || np == 4;
      const int lead = newunit ? 4 : 1;
      WAIT_LGKM(3);
      SB();
#pragma unroll
      for (int i = 0; i < 16; ++i) {
        const int m = i >> 2, n = (m & 1) ? 3 - (i & 3) : (i & 3);
        if (i == 4 && !newunit) { WAIT_LGKM(4); SB(); }
        acc[m][n] = __builtin_amdgcn_mfma_f32_16x16x32_bf16(cur.a[m], cat(cur.b[n][0], cur.b[n][1]), acc[m][n], 0, 0, 0);
        SB();
        if (newunit && i == lead - 1) {
          WAIT_LGKM(0);
          bar();
          SB();
        }
        if (i >= lead - 1 && i < lead - 1 + 12) { rd(nxt, gs + 1, i - (lead - 1)); SB(); }
      }
    }
    WAIT_LGKM(3);
    SB();
    sfor<16>([&](auto ic) __attribute__((always_inline)) {
      constexpr int i = decltype(ic)::value, m = i >> 2, n = (m & 1) ? 3 - (i & 3) : (i & 3);
      acc[m][n] = __builtin_amdgcn_mfma_f32_16x16x32_bf16(fr[1].a[m], cat(fr[1].b[n][0], fr[1].b[n][1]), acc[m][n], 0, 0, 0);
      SB();
      if constexpr (i == 3) { WAIT_LGKM(0); bar(); SB(); }
      if constexpr (i >= 3) { rdT(CI(i - 3)); SB(); }
    });
    u32 cnt0; f32x4 ra, rb; u32x2 ep;
    sfor<96>([&](auto gc) __attribute__((always_inline)) {
      constexpr int gidx = decltype(gc)::value, e = gidx >> 3, i = gidx & 7, j = TJ(e), h = TH(e);
      constexpr int m = i >> 1, n = 2 * h + ((i ^ (i >> 1)) & 1);
      acc[m][n] = __builtin_amdgcn_mfma_f32_16x16x32_bf16(tf[j].a[m], cat(tf[j].b[n][0], tf[j].b[n][1]), acc[m][n], 0, 0, 0);
      SB();
      if constexpr (gidx < 23) { rdT(CI(13 + gidx)); SB(); }
      if constexpr (gidx == 23) { ep = *(const LAS u32x2*)(uintptr_t)(flga + 32); SB(); }
      if constexpr (gidx == 44) { tagA = ep.x + 1u; tagB = ep.y + 1u; PIN(tagA); PIN(tagB); SB(); }
      if constexpr (gidx == 24) { bar(); SB(); }
      if constexpr (gidx >= 24 && gidx < 60) { rdT(CI(36 + gidx - 24)); SB(); }
      if constexpr (gidx >= G_ST && gidx < G_ST + 8) { constexpr int k = gidx - G_ST; stat(k >> 1, (k ^ (k >> 1)) & 1, 0); stat(k >> 1, (k ^ (k >> 1)) & 1, 1); SB(); }
      if constexpr (gidx == G_ST + 9) {
        const float pr = red_half(0);
        asm volatile("ds_write_b32 %3, %4\n\t"
                     "s_mov_b64 exec, 1\n\t"
                     "ds_add_rtn_u32 %0, %5, %6\n\t"
                     "s_mov_b64 exec, -1\n\t"
                     "ds_read2st64_b64 %1, %7 offset1:1\n\t"
                     "ds_read2st64_b64 %2, %7 offset0:2 offset1:3"
                     : "=&v"(cnt0), "=&v"(ra), "=&v"(rb) : "v"(redw), "v"(pr), "v"(flga), "v"(one), "v"(redo) : "memory");
        SB();
      }
      if constexpr (gidx == G_ST + 9 + G_W) {
        asm volatile("s_waitcnt lgkmcnt(0)" : "+v"(cnt0), "+v"(ra), "+v"(rb) :: "memory");
        const f32x2 rr = (ra.xy + ra.zw) + (rb.xy + rb.zw);
        u32x4 gv = {__float_as_uint(rr.x), tagA, __float_as_uint(rr.y), tagB};
        __builtin_amdgcn_raw_buffer_store_b128(gv, gr, __builtin_amdgcn_readfirstlane(cnt0) == 3u ? gvo : OOB, 0, 16);
        SB();
      }
      if constexpr (gidx >= 92) { constexpr int k = 2 * (gidx - 92); stat(k >> 2, 2 + (((k >> 1) ^ (k >> 2)) & 1), 0); stat(k >> 2, 2 + (((k >> 1) ^ (k >> 2)) & 1), 1); SB(); }
    });
#pragma unroll
    for (int k = 8; k < 16; ++k) stat(k >> 2, 2 + (((k >> 1) ^ (k >> 2)) & 1), k & 1);
    {
      const float pr = red_half(1);
      u32 old1, c0; f32x4 r01, r23;
      asm volatile("ds_write_b32 %4, %5 offset:256\n\t"
                   "s_mov_b64 exec, 1\n\t"
                   "ds_add_rtn_u32 %0, %6, %7 offset:4\n\t"
                   "s_mov_b64 exec, -1\n\t"
                   "ds_read2st64_b64 %1, %8 offset1:1\n\t"
                   "ds_read2st64_b64 %2, %8 offset0:2 offset1:3\n\t"
                   "ds_read_b32 %3, %6\n\t"
                   "s_waitcnt lgkmcnt(0)"
                   : "=&v"(old1), "=&v"(r01), "=&v"(r23), "=&v"(c0) : "v"(redw), "v"(pr), "v"(flga), "v"(one), "v"(redo1) : "memory");
      const f32x2 rr = (r01.xy + r01.zw) + (r23.xy + r23.zw);
      u32x4 gv = {__float_as_uint(rr.x), tagA, __float_as_uint(rr.y), tagB};
      __builtin_amdgcn_raw_buffer_store_b128(gv, gr, __builtin_amdgcn_readfirstlane(old1) == 3u ? gvo1 : OOB, 0, 16);
      __builtin_amdgcn_s_setprio(0);
      if ((u32)__builtin_amdgcn_readfirstlane(c0) < 4u) lds_wait_ge(FLG_OFF, 4u);
    }
#pragma unroll
    for (int hh = 0; hh < 2; ++hh)
#pragma unroll
      for (int m = 0; m < 4; ++m)
#pragma unroll
        for (int j = 0; j < 4; ++j)
#pragma unroll
          for (int n = 2 * hh; n < 2 * hh + 2; ++n)
            *(LAS float*)(uintptr_t)(((j & 1) ? hw1 : hw0) + (16 * m + 2 * j) * 256 + (n ^ (j & 2)) * 64) = acc[m][n][j];
    asm volatile("ds_write_b32 %0, %1" :: "v"(FLG_OFF + 16 + wq * 4), "v"(one) : "memory");
#pragma unroll
    for (int i = 0; i < 4; ++i) { hv[i][0] = *(const LAS f32x4*)(uintptr_t)(h0 + i * 4096); hv[i][1] = *(const LAS f32x4*)(uintptr_t)(h1 + i * 4096); }
  }
  f32x4 av[2], bv[2];
#pragma unroll 1
  for (u32 sp = 0; sp < (1u << 24); ++sp) {
    u32 c;
    asm volatile("ds_read_b32 %0, %5\n\tds_read_b128 %1, %6\n\tds_read_b128 %2, %6 offset:16\n\tds_read_b128 %3, %6 offset:256\n\tds_read_b128 %4, %6 offset:272\n\ts_waitcnt lgkmcnt(0)"
                 : "=&v"(c), "=&v"(av[0]), "=&v"(av[1]), "=&v"(bv[0]), "=&v"(bv[1]) : "v"(abf), "v"(abo) : "memory");
    if ((u32)__builtin_amdgcn_readfirstlane(c) >= 2u) break;
  }
  const __amdgpu_buffer_rsrc_t orr = __builtin_amdgcn_make_buffer_rsrc((void*)outp, 0, (int)(xlen >> 1) - 640, 0x00020000);
  const float K1 = -2.0f * 0.7978845608028654f * 1.4426950408889634f;
  const float K3 = K1 * 0.044715f;
  const f32x2 K1v = {K1, K1}, K3v = {K3, K3}, ONE = {1.f, 1.f};
#pragma unroll
  for (int i = 0; i < 4; ++i) {
    f32x2 z[4], e[4];
#pragma unroll
    for (int j = 0; j < 4; ++j) {
      const f32x2 h2 = (j & 1) ? hv[i][j >> 1].zw : hv[i][j >> 1].xy;
      const f32x2 a2 = (j & 1) ? av[j >> 1].zw : av[j >> 1].xy;
      const f32x2 b2 = (j & 1) ? bv[j >> 1].zw : bv[j >> 1].xy;
      z[j] = __builtin_elementwise_fma(h2, a2, b2);
      e[j] = z[j] * __builtin_elementwise_fma(z[j] * z[j], K3v, K1v);
    }
#pragma unroll
    for (int j = 0; j < 4; ++j) { e[j].x = __builtin_amdgcn_exp2f(e[j].x); e[j].y = __builtin_amdgcn_exp2f(e[j].y); }
#pragma unroll
    for (int j = 0; j < 4; ++j) e[j] += ONE;
#pragma unroll
    for (int j = 0; j < 4; ++j) { e[j].x = __builtin_amdgcn_rcpf(e[j].x); e[j].y = __builtin_amdgcn_rcpf(e[j].y); }
    u32x4 o;
#pragma unroll
    for (int j = 0; j < 4; ++j) {
      const f32x2 y = z[j] * e[j];
      bf16x2 pk = {(__bf16)y.x, (__bf16)y.y};
      o[j] = __builtin_bit_cast(u32, pk);
    }
    __builtin_amdgcn_raw_buffer_store_b128(o, orr, ov + i * 16 * 512, 0, 16);
  }
}

extern "C" void kernel_launch(void* const* d_in, const int* in_sizes, int n_in, void* d_out, int out_size,
                              void* d_ws, size_t ws_size, hipStream_t stream) {
  (void)in_sizes; (void)n_in; (void)out_size; (void)ws_size;
  (void)hipFuncSetAttribute((const void*)conv_stem_kernel, hipFuncAttributeMaxDynamicSharedMemorySize, LDS_BYTES);
  hipLaunchKernelGGL(conv_stem_kernel, dim3(256), dim3(512), LDS_BYTES, stream, (const u16*)d_in[0], (const u16*)d_in[1],
                     (const u16*)d_in[2], (const u16*)d_in[3], (u16*)d_out, (char*)d_ws);
}
```
